```python
import torch

def _softmax(x, axis=-1):
    return torch.softmax(x, dim=axis)
def _gelu(x):
    return torch.nn.functional.gelu(x, approximate="tanh")
def _silu(x):
    return torch.nn.functional.silu(x)
def _sigmoid(x):
    return torch.sigmoid(x)
def _layernorm(x, axis=-1, eps=1e-5):
    m = x.mean(dim=axis, keepdim=True)
    v = x.var(dim=axis, keepdim=True, unbiased=False)
    return (x - m) / torch.sqrt(v + eps)
def _scatter_add(tgt, idx, src, axis=0):
    return tgt.clone().index_add_(axis, idx, src)
def _segment_sum(src, seg, n):
    out = torch.zeros((n,) + src.shape[1:], dtype=src.dtype)
    return out.index_add_(0, seg, src)
def _pad(x, pw):
    flat = tuple(v for pair in reversed(pw) for v in pair)
    return torch.nn.functional.pad(x, flat)

kernel_name = 'gated_mix__sliding_local_dilated__spectral_fnet__prompt'

def setup_inputs():

    _w = (lambda a: (lambda t: t.to(torch.bfloat16) if t.is_floating_point() else t)(torch.as_tensor(a)))
    def _A_setup():
        B, S, D = 1, 2048, 1024
        return {
            "x": _w((lambda *s: torch.randn(*s, dtype=torch.bfloat16) * 0.05)(B, S, D)),
            "wqkv": _w((lambda *s: torch.randn(*s, dtype=torch.bfloat16) * 1.6)(D, 3 * D)),
            "wo": _w((lambda *s: torch.randn(*s, dtype=torch.bfloat16) * 0.2)(D, D)),
        }
    _A_in = _A_setup()

    def _B_setup():
        B, S, D = 1, 2048, 1024
        ns = torch.arange(S)[:, None] * torch.arange(S)[None, :]
        nd = torch.arange(D)[:, None] * torch.arange(D)[None, :]
        Ws_re = torch.cos(-2.0 * torch.pi * ns / S).to(float)
        Ws_im = torch.sin(-2.0 * torch.pi * ns / S).to(float)
        Wd_re = torch.cos(-2.0 * torch.pi * nd / D).to(float)
        Wd_im = torch.sin(-2.0 * torch.pi * nd / D).to(float)
        return {
            "x": _w((lambda *s: torch.randn(*s, dtype=torch.bfloat16) * 0.05)(B, S, D)),
            "dft_seq_re": _w(Ws_re),
            "dft_seq_im": _w(Ws_im),
            "dft_hid_re": _w(Wd_re),
            "dft_hid_im": _w(Wd_im),
        }
    _B_in = _B_setup()

    return {
        "A_x": _A_in["x"],
        "A_wqkv": _A_in["wqkv"],
        "A_wo": _A_in["wo"],
        "B_x": _B_in["x"],
        "B_dft_seq_re": _B_in["dft_seq_re"],
        "B_dft_seq_im": _B_in["dft_seq_im"],
        "B_dft_hid_re": _B_in["dft_hid_re"],
        "B_dft_hid_im": _B_in["dft_hid_im"],
        "gate_w": _w((lambda *s: torch.randn(*s, dtype=torch.bfloat16) * 0.05)(1024) * 16.0),
    }

def reference(A_x, A_wqkv, A_wo, B_x, B_dft_seq_re, B_dft_seq_im, B_dft_hid_re, B_dft_hid_im, gate_w):
    def _A_ref(x, wqkv, wo):
        B, S, D = x.shape
        H = 32; Dh = D // H
        W, dil = 8, 2
        qkv = (lambda x, *d: x.permute(*d))((x @ wqkv).reshape(B, S, 3, H, Dh), 2, 0, 3, 1, 4)
        q, k, v = qkv[0], qkv[1], qkv[2]
        scale = Dh ** -0.5
        pos = torch.arange(S)
        off = torch.arange(W) * dil
        idx = pos[:, None] - off[None, :]
        mask = idx >= 0
        idx_c = torch.where(mask, idx, 0).reshape(-1)
        kg = k[:, :, idx_c, :].reshape(B, H, S, W, Dh)
        vg = v[:, :, idx_c, :].reshape(B, H, S, W, Dh)
        logits = torch.einsum("bhsd,bhswd->bhsw", q, kg) * scale
        logits = torch.where(mask[None, None, :, :], logits, -1e9)
        attn = _softmax(logits, axis=-1)
        o = torch.einsum("bhsw,bhswd->bhsd", attn, vg)
        return (lambda x, *d: x.permute(*d))(o, 0, 2, 1, 3).reshape(B, S, D) @ wo

    def _B_ref(x, dft_seq_re, dft_seq_im, dft_hid_re, dft_hid_im):
        a = torch.einsum("bsd,ed->bse", x, dft_hid_re)
        b = torch.einsum("bsd,ed->bse", x, dft_hid_im)
        y = torch.einsum("ts,bse->bte", dft_seq_re, a) - torch.einsum("ts,bse->bte", dft_seq_im, b)
        return y

    x = A_x
    g = _sigmoid((x * gate_w).sum(-1, keepdims=True))
    a = _A_ref(x, A_wqkv, A_wo)
    b = _B_ref(x, B_dft_seq_re, B_dft_seq_im, B_dft_hid_re, B_dft_hid_im)
    return g * a + (1 - g) * b
```

```cpp
#include <hip/hip_runtime.h>
#include <hip/hip_bf16.h>
typedef unsigned short u16;
__device__ __forceinline__ float b2f(u16 b){ return __uint_as_float(((unsigned)b)<<16); }
__device__ __forceinline__ u16 f2b(float f){ unsigned u=__float_as_uint(f); u += 0x7FFFu + ((u>>16)&1u); return (u16)(u>>16); }
__device__ __forceinline__ float rb(float f){ return b2f(f2b(f)); }
#define MIMIC 1
template<bool BT>
__global__ void gemm_naive(const u16* A, const u16* B, float* C, int M, int N, int K){
  __shared__ float As[32][33], Bs[32][33];
  int tx=threadIdx.x, ty=threadIdx.y; int m=blockIdx.y*32+ty, n=blockIdx.x*32+tx;
  float acc=0;
  for(int k0=0;k0<K;k0+=32){
    As[ty][tx]=b2f(A[(size_t)m*K+k0+tx]);
    if(BT) Bs[tx][ty]=b2f(B[(size_t)(blockIdx.x*32+ty)*K+k0+tx]);
    else Bs[ty][tx]=b2f(B[(size_t)(k0+ty)*N+n]);
    __syncthreads();
    for(int k=0;k<32;++k) acc+=As[ty][k]*Bs[k][tx];
    __syncthreads();
  }
  C[(size_t)m*N+n]=acc;
}
__global__ void cvt(const float* in, u16* out, size_t n){ size_t i=(size_t)blockIdx.x*blockDim.x+threadIdx.x; if(i<n) out[i]=f2b(in[i]); }
__global__ void gate_k(const u16* x, const u16* gw, float* g){
  int r=blockIdx.x; int l=threadIdx.x; float s=0;
  for(int d=l; d<1024; d+=64){ float p=b2f(x[r*1024+d])*b2f(gw[d]); s+= MIMIC? rb(p):p; }
  for(int o=32;o>=1;o>>=1) s+=__shfl_xor(s,o);
  if(l==0){ if(MIMIC) s=rb(s); float v=1.f/(1.f+expf(-s)); g[r]= MIMIC? rb(v):v; }
}
__global__ void attn_k(const u16* qkv, u16* o){
  int s=blockIdx.x, h=threadIdx.x;
  const u16* q=qkv+(size_t)s*3072+h*32;
  float lg[8]; float mx=-1e30f;
  for(int w=0;w<8;++w){ int j=s-2*w; if(j>=0){ const u16* k=qkv+(size_t)j*3072+1024+h*32; float d=0; for(int i=0;i<32;++i) d+=b2f(q[i])*b2f(k[i]);
      if(MIMIC) d=rb(rb(d)*0.17677669529663687f); else d*=0.17677669529663687f; lg[w]=d;} else lg[w]=-1e9f; mx=fmaxf(mx,lg[w]); }
  float sum=0; for(int w=0;w<8;++w){ lg[w]=expf(lg[w]-mx); sum+=lg[w]; }
  for(int w=0;w<8;++w){ lg[w]/=sum; if(MIMIC) lg[w]=rb(lg[w]); }
  for(int i=0;i<32;++i){ float a=0; for(int w=0;w<8;++w){ int j=s-2*w; if(j<0) j=0; a+=lg[w]*b2f(qkv[(size_t)j*3072+2048+h*32+i]); } o[(size_t)s*1024+h*32+i]=f2b(a); }
}
__global__ void fin_k(const float* a, const float* y1, const float* y2, const float* g, u16* out){
  size_t i=(size_t)blockIdx.x*blockDim.x+threadIdx.x; int r=i>>10; float gg=g[r];
  if(MIMIC){ float y=rb(rb(y1[i])-rb(y2[i])); out[i]=f2b(rb(gg*rb(a[i]))+rb(rb(1.f-gg)*y)); }
  else out[i]=f2b(gg*a[i]+(1.f-gg)*(y1[i]-y2[i]));
}
extern "C" void kernel_launch(void* const* d_in, const int* in_sizes, int n_in, void* d_out, int out_size, void* d_ws, size_t ws_size, hipStream_t stream){
  const u16* x=(const u16*)d_in[0]; const u16* wqkv=(const u16*)d_in[1]; const u16* wo=(const u16*)d_in[2];
  const u16* cr=(const u16*)d_in[4]; const u16* ci=(const u16*)d_in[5]; const u16* hr=(const u16*)d_in[6]; const u16* hi=(const u16*)d_in[7]; const u16* gw=(const u16*)d_in[8];
  char* ws=(char*)d_ws;
  float* f0=(float*)ws; ws+=(size_t)2048*3072*4;
  float* f1=(float*)ws; ws+=(size_t)2048*1024*4;
  float* f2=(float*)ws; ws+=(size_t)2048*1024*4;
  float* f3=(float*)ws; ws+=(size_t)2048*1024*4;
  u16* qkv=(u16*)ws; ws+=(size_t)2048*3072*2;
  u16* o=(u16*)ws; ws+=(size_t)2048*1024*2;
  u16* a=(u16*)ws; ws+=(size_t)2048*1024*2;
  u16* b=(u16*)ws; ws+=(size_t)2048*1024*2;
  float* g=(float*)ws;
  dim3 blk(32,32);
  gate_k<<<2048,64,0,stream>>>(x,gw,g);
  gemm_naive<false><<<dim3(3072/32,2048/32),blk,0,stream>>>(x,wqkv,f0,2048,3072,1024);
  cvt<<<2048*3072/256,256,0,stream>>>(f0,qkv,(size_t)2048*3072);
  attn_k<<<2048,32,0,stream>>>(qkv,o);
  gemm_naive<false><<<dim3(1024/32,2048/32),blk,0,stream>>>(o,wo,f1,2048,1024,1024);
  gemm_naive<true><<<dim3(1024/32,2048/32),blk,0,stream>>>(x,hr,f2,2048,1024,1024);
  cvt<<<2048*1024/256,256,0,stream>>>(f2,a,(size_t)2048*1024);
  gemm_naive<true><<<dim3(1024/32,2048/32),blk,0,stream>>>(x,hi,f2,2048,1024,1024);
  cvt<<<2048*1024/256,256,0,stream>>>(f2,b,(size_t)2048*1024);
  gemm_naive<false><<<dim3(1024/32,2048/32),blk,0,stream>>>(cr,a,f2,2048,1024,2048);
  gemm_naive<false><<<dim3(1024/32,2048/32),blk,0,stream>>>(ci,b,f3,2048,1024,2048);
  fin_k<<<2048*1024/256,256,0,stream>>>(f1,f2,f3,g,(u16*)d_out);
}
```

```cpp
#include <hip/hip_runtime.h>
#include <hip/hip_bf16.h>
typedef unsigned short u16;
__device__ __forceinline__ float b2f(u16 b){ return __uint_as_float(((unsigned)b)<<16); }
__device__ __forceinline__ u16 f2b(float f){ unsigned u=__float_as_uint(f); u += 0x7FFFu + ((u>>16)&1u); return (u16)(u>>16); }
__device__ __forceinline__ float rb(float f){ return b2f(f2b(f)); }
#define MIMIC 1
template<bool BT>
__global__ void gemm_naive(const u16* A, const u16* B, float* C, int M, int N, int K){
  __shared__ float As[32][33], Bs[32][33];
  int tx=threadIdx.x, ty=threadIdx.y; int m=blockIdx.y*32+ty, n=blockIdx.x*32+tx;
  float acc=0;
  for(int k0=0;k0<K;k0+=32){
    As[ty][tx]=b2f(A[(size_t)m*K+k0+tx]);
    if(BT) Bs[tx][ty]=b2f(B[(size_t)(blockIdx.x*32+ty)*K+k0+tx]);
    else Bs[ty][tx]=b2f(B[(size_t)(k0+ty)*N+n]);
    __syncthreads();
    for(int k=0;k<32;++k) acc+=As[ty][k]*Bs[k][tx];
    __syncthreads();
  }
  C[(size_t)m*N+n]=acc;
}
__global__ void cvt(const float* in, u16* out, size_t n){ size_t i=(size_t)blockIdx.x*blockDim.x+threadIdx.x; if(i<n) out[i]=f2b(in[i]); }
__global__ void gate_k(const u16* x, const u16* gw, float* g){
  int r=blockIdx.x; int l=threadIdx.x; float s=0;
  for(int d=l; d<1024; d+=64){ float p=b2f(x[r*1024+d])*b2f(gw[d]); s+= MIMIC? rb(p):p; }
  for(int o=32;o>=1;o>>=1) s+=__shfl_xor(s,o);
  if(l==0){ if(MIMIC) s=rb(s); float v=1.f/(1.f+expf(-s)); g[r]= MIMIC? rb(v):v; }
}
__global__ void attn_k(const u16* qkv, u16* o){
  int s=blockIdx.x, h=threadIdx.x;
  const u16* q=qkv+(size_t)s*3072+h*32;
  float lg[8]; float mx=-1e30f;
  for(int w=0;w<8;++w){ int j=s-2*w; if(j>=0){ const u16* k=qkv+(size_t)j*3072+1024+h*32; float d=0; for(int i=0;i<32;++i) d+=b2f(q[i])*b2f(k[i]);
      if(MIMIC) d=rb(rb(d)*0.17677669529663687f); else d*=0.17677669529663687f; lg[w]=d;} else lg[w]=-1e9f; mx=fmaxf(mx,lg[w]); }
  float sum=0; for(int w=0;w<8;++w){ lg[w]=expf(lg[w]-mx); sum+=lg[w]; }
  for(int w=0;w<8;++w){ lg[w]/=sum; if(MIMIC) lg[w]=rb(lg[w]); }
  for(int i=0;i<32;++i){ float a=0; for(int w=0;w<8;++w){ int j=s-2*w; if(j<0) j=0; a+=lg[w]*b2f(qkv[(size_t)j*3072+2048+h*32+i]); } o[(size_t)s*1024+h*32+i]=f2b(a); }
}
__global__ void fin_k(const float* a, const float* y1, const float* y2, const float* g, u16* out){
  size_t i=(size_t)blockIdx.x*blockDim.x+threadIdx.x; int r=i>>10; float gg=g[r];
  if(MIMIC){ float y=rb(rb(y1[i])-rb(y2[i])); out[i]=f2b(rb(gg*rb(a[i]))+rb(rb(1.f-gg)*y)); }
  else out[i]=f2b(gg*a[i]+(1.f-gg)*(y1[i]-y2[i]));
}

typedef unsigned u32x4 __attribute__((ext_vector_type(4)));
__global__ __launch_bounds__(256) void probe(const char* p, size_t bytes, unsigned* sink){
  size_t per=bytes/gridDim.x; const char* b=p+per*blockIdx.x; u32x4 a={0,0,0,0};
  for(size_t o=threadIdx.x*16;o<per;o+=256*16*8){
    u32x4 v[8];
    #pragma unroll
    for(int i=0;i<8;++i) v[i]=*(const u32x4*)(b+o+i*4096);
    #pragma unroll
    for(int i=0;i<8;++i) a^=v[i];
  }
  sink[blockIdx.x*256+threadIdx.x]=a.x^a.y^a.z^a.w;
}
extern "C" void kernel_launch(void* const* d_in, const int* in_sizes, int n_in, void* d_out, int out_size, void* d_ws, size_t ws_size, hipStream_t stream){
  const u16* x=(const u16*)d_in[0]; const u16* wqkv=(const u16*)d_in[1]; const u16* wo=(const u16*)d_in[2];
  const u16* cr=(const u16*)d_in[4]; const u16* ci=(const u16*)d_in[5]; const u16* hr=(const u16*)d_in[6]; const u16* hi=(const u16*)d_in[7]; const u16* gw=(const u16*)d_in[8];
  char* ws=(char*)d_ws;
  float* f0=(float*)ws; ws+=(size_t)2048*3072*4;
  float* f1=(float*)ws; ws+=(size_t)2048*1024*4;
  float* f2=(float*)ws; ws+=(size_t)2048*1024*4;
  float* f3=(float*)ws; ws+=(size_t)2048*1024*4;
  u16* qkv=(u16*)ws; ws+=(size_t)2048*3072*2;
  u16* o=(u16*)ws; ws+=(size_t)2048*1024*2;
  u16* a=(u16*)ws; ws+=(size_t)2048*1024*2;
  u16* b=(u16*)ws; ws+=(size_t)2048*1024*2;
  float* g=(float*)ws;
  dim3 blk(32,32);
  unsigned* sink=(unsigned*)((char*)d_ws+(200<<20));
  probe<<<64,256,0,stream>>>((const char*)d_in[0],(size_t)4<<20,sink);
  probe<<<64,256,0,stream>>>((const char*)d_in[1],(size_t)6<<20,sink);
  probe<<<64,256,0,stream>>>((const char*)d_in[2],(size_t)2<<20,sink);
  probe<<<64,256,0,stream>>>((const char*)d_in[4],(size_t)8<<20,sink);
  probe<<<64,256,0,stream>>>((const char*)d_in[5],(size_t)8<<20,sink);
  probe<<<64,256,0,stream>>>((const char*)d_in[6],(size_t)2<<20,sink);
  probe<<<64,256,0,stream>>>((const char*)d_in[7],(size_t)2<<20,sink);
  gate_k<<<2048,64,0,stream>>>(x,gw,g);
  gemm_naive<false><<<dim3(3072/32,2048/32),blk,0,stream>>>(x,wqkv,f0,2048,3072,1024);
  cvt<<<2048*3072/256,256,0,stream>>>(f0,qkv,(size_t)2048*3072);
  attn_k<<<2048,32,0,stream>>>(qkv,o);
  gemm_naive<false><<<dim3(1024/32,2048/32),blk,0,stream>>>(o,wo,f1,2048,1024,1024);
  gemm_naive<true><<<dim3(1024/32,2048/32),blk,0,stream>>>(x,hr,f2,2048,1024,1024);
  cvt<<<2048*1024/256,256,0,stream>>>(f2,a,(size_t)2048*1024);
  gemm_naive<true><<<dim3(1024/32,2048/32),blk,0,stream>>>(x,hi,f2,2048,1024,1024);
  cvt<<<2048*1024/256,256,0,stream>>>(f2,b,(size_t)2048*1024);
  gemm_naive<false><<<dim3(1024/32,2048/32),blk,0,stream>>>(cr,a,f2,2048,1024,2048);
  gemm_naive<false><<<dim3(1024/32,2048/32),blk,0,stream>>>(ci,b,f3,2048,1024,2048);
  fin_k<<<2048*1024/256,256,0,stream>>>(f1,f2,f3,g,(u16*)d_out);
}
```

```cpp
#include <hip/hip_runtime.h>
typedef unsigned short u16;
typedef unsigned long long u64;
typedef unsigned u32x4 __attribute__((ext_vector_type(4)));
typedef int i32x4 __attribute__((ext_vector_type(4)));
typedef float f32x4 __attribute__((ext_vector_type(4)));
typedef float f32x32 __attribute__((ext_vector_type(32)));
typedef __attribute__((address_space(1))) u64 gu64;
#define LAS __attribute__((address_space(3)))
#define RLX_AGENT __ATOMIC_RELAXED, __HIP_MEMORY_SCOPE_AGENT
#define BAR() asm volatile("s_waitcnt lgkmcnt(0)\n\ts_barrier" ::: "memory")
#define WAITV(n) asm volatile("s_waitcnt vmcnt(%0)" ::"n"(n) : "memory")

#define NOQ 12
#define NPI 13
#define PSPIN 2200
#define CODE_BYTES 36864
#define LEAD 4
constexpr unsigned WS_EPOCH = 0, WS_ACK = 1024, WS_G = 32768, WS_MB = 65536, WS_A2 = 1u << 20, WS_PART = 13u << 20;
constexpr unsigned L_PB = 98304, L_QH = 98304, L_QB = 106496, L_SB = 65536, L_X = 131072, L_G = 155648, L_T = 156672, L_TAG = 156928, L_POLL = 157184, L_TOTAL = 163840;

typedef float f32x2 __attribute__((ext_vector_type(2)));
typedef __bf16 bf2 __attribute__((ext_vector_type(2)));
__device__ __forceinline__ unsigned cvtpk(float lo, float hi) { return __builtin_bit_cast(unsigned, __builtin_convertvector((f32x2){lo, hi}, bf2)); }
__device__ __forceinline__ float bflo(unsigned v) { return __uint_as_float(v << 16); }
__device__ __forceinline__ float rbf(float f) { return bflo(cvtpk(f, f)); }
__device__ __forceinline__ i32x4 mksrd(const void* p, unsigned bytes) { u64 a = (u64)p; i32x4 r; r.x = (int)(unsigned)a; r.y = (int)((unsigned)(a >> 32) & 0xffffu); r.z = (int)bytes; r.w = 0x00020000; return r; }
#define DMA_(P, O) "buffer_load_dwordx4 %[v" #P "], %[d], %[s] offen offset:" #O
#define DMA4_(X) asm volatile("s_mov_b32 m0, %[l]\n\ts_nop 0\n\t" DMA_(0, 0) X "\n\t" DMA_(1, 1024) X "\n\t" DMA_(2, 2048) X "\n\t" DMA_(3, 3072) X \
    :: [l] "s"(lds), [v0] "v"(v0), [v1] "v"(v1), [v2] "v"(v2), [v3] "v"(v3), [d] "s"(srd), [s] "s"(soff) : "memory")
#define DMA4N_(X) asm volatile("s_mov_b32 m0, %[l]\n\ts_nop 3\n\t" DMA_(0, 0) X "\n\t" DMA_(1, 1024) X "\n\t" DMA_(2, 2048) X "\n\t" DMA_(3, 3072) X \
    :: [l] "s"(lds), [v0] "v"(v0), [v1] "v"(v1), [v2] "v"(v2), [v3] "v"(v3), [d] "s"(srd), [s] "s"(soff) : "memory")
template <int POL> __device__ __forceinline__ void dma4n(unsigned lds, unsigned v0, unsigned v1, unsigned v2, unsigned v3, i32x4 srd, unsigned soff) {
  if (POL == 2) DMA4N_(" sc1 lds"); else DMA4N_(" lds");
}
template <int POL> __device__ __forceinline__ void dma4(unsigned lds, unsigned v0, unsigned v1, unsigned v2, unsigned v3, i32x4 srd, unsigned soff) {
  if (POL == 2) DMA4_(" sc1 lds"); else DMA4_(" lds");
}
__device__ __forceinline__ void dma2(unsigned lds, unsigned v0, unsigned v1, i32x4 srd, unsigned soff) {
  asm volatile("s_mov_b32 m0, %[l]\n\ts_nop 0\n\t" DMA_(0, 0) " lds\n\t" DMA_(1, 1024) " lds" :: [l] "s"(lds), [v0] "v"(v0), [v1] "v"(v1), [d] "s"(srd), [s] "s"(soff) : "memory");
}
__device__ __forceinline__ void dma_half(unsigned lds, unsigned voff, i32x4 srd, unsigned soff) {
  asm volatile("s_mov_b32 m0, %0\n\ts_mov_b32 exec_hi, 0\n\tbuffer_load_dwordx4 %1, %2, %3 offen lds\n\ts_mov_b32 exec_hi, -1" ::"s"(lds), "v"(voff), "s"(srd), "s"(soff) : "memory");
}
__device__ __forceinline__ float dot2(unsigned a, unsigned b, float c) { return __builtin_amdgcn_fdot2_f32_bf16(__builtin_bit_cast(bf2, a), __builtin_bit_cast(bf2, b), c, false); }
template <int CTL> __device__ __forceinline__ float dpp_add(float v) { return v + __builtin_bit_cast(float, __builtin_amdgcn_mov_dpp(__builtin_bit_cast(int, v), CTL, 0xf, 0xf, true)); }
__device__ __forceinline__ int lead(int s, int j, int D, int T) {
  const u64 m = __ballot(((s + (s >> 3) - j) & 7) == 0 && s >= D && s < T) >> s;
  return (m >> D) & 1 ? s + D : m & ((1 << D) - 1) ? s - 1 : s;
}
__device__ __forceinline__ u64 mktag(u64 e) { return e * 0x9E3779B97F4A7C15ull + 0xD1B54A32D192ED03ull; }

__device__ __forceinline__ float gate2(const u32x4* gx, const u32x4* gg) {
  float s0 = 0.f, s1 = 0.f;
#pragma unroll
  for (int e = 0; e < 8; ++e) { s0 = dot2(gx[e >> 2][e & 3], gg[e >> 2][e & 3], s0); s1 = dot2(gx[2 + (e >> 2)][e & 3], gg[e >> 2][e & 3], s1); }
  auto sw = __builtin_amdgcn_permlane32_swap(__float_as_uint(s0), __float_as_uint(s1), false, false);
  float t = __uint_as_float(sw[0]) + __uint_as_float(sw[1]);
  sw = __builtin_amdgcn_permlane16_swap(__float_as_uint(t), __float_as_uint(t), false, false);
  t = dpp_add<0x121>(dpp_add<0x122>(dpp_add<0x124>(dpp_add<0x128>(__uint_as_float(sw[0]) + __uint_as_float(sw[1])))));
  return rbf(__builtin_amdgcn_rcpf(1.0f + __builtin_amdgcn_exp2f(-1.4426950408889634f * rbf(t))));
}
#define LANES int lane; asm volatile("v_mbcnt_lo_u32_b32 %0, -1, 0\n\tv_mbcnt_hi_u32_b32 %0, -1, %0" : "=v"(lane)); \
  const int g = (lane >> 4) & 3, l15 = lane & 15, q = (lane >> 2) & 3, p = lane & 3; const unsigned f = ((q >> 1) & 1) | ((g & 1) << 1); (void)p; (void)f; (void)l15

__global__ __launch_bounds__(512, 2) void gmix(const u16* __restrict__ x, const u16* __restrict__ wqkv, const u16* __restrict__ wo, const u16* __restrict__ cr,
                                              const u16* __restrict__ ci, const u16* __restrict__ hr, const u16* __restrict__ hi, const u16* __restrict__ gw,
                                              u16* __restrict__ out, char* __restrict__ ws) {
  __shared__ __attribute__((aligned(16))) char smem[L_TOTAL];
  asm volatile("" ::"s"((unsigned)(size_t)(LAS char*)smem) : "memory");
  const int b = blockIdx.x, idx = b >> 3;
  if ((threadIdx.x >> 4) == 16) {
    u64 pc0;
    asm volatile("s_getpc_b64 %0" : "=s"(pc0));
    const unsigned co = (idx + (threadIdx.x & 15) * 32) * 128;
    asm volatile("s_mov_b32 m0, %0\n\ts_nop 0\n\tglobal_load_lds_dword %1, %2" ::"s"(L_T), "v"(co < CODE_BYTES ? co : 0u), "s"(pc0 & ~255ull) : "memory");
  }
  const bool isP = idx < 16;
  const int hf = (b >> 1) & 1, xg = (((b >> 1) & 2) | (b & 1)) ^ ((idx >> 4) * 3);
  const int tm = xg * 4 + (idx & 3), tn = hf * 4 + ((idx >> 2) & 3);
  const int pan = tm >> 1, hp = tn * 2 + (tm & 1);
  const int hw = __builtin_amdgcn_readfirstlane(threadIdx.x >> 6), wid = hw < 4 ? hw + 8 : hw - 4;
  asm volatile("" ::"s"(x), "s"(wqkv), "s"(wo), "s"(cr), "s"(ci), "s"(hr), "s"(hi), "s"(gw), "s"(out), "s"(ws));
  const int jrot = isP ? tn + tm : pan + hp, sh = isP ? 12 : 11;
  i32x4 sA0 = mksrd(isP ? cr : x, isP ? 2048 * 4096 : 2048 * 2048), sA1 = mksrd(isP ? ci : x, isP ? 2048 * 4096 : 2048 * 2048);
  unsigned oA0 = (isP ? tm * 128 + (hw & 3) * 32 : pan * 256 + (hw & 3) * 64) << sh, oA1 = oA0 + (isP ? 0 : 32 * 2048);
  unsigned lA0 = (hw & 3) * (isP ? 4096 : 24576), lA1 = lA0 + (isP ? 49152 : 4096);
  if (hw < 4) {
    __builtin_amdgcn_s_setprio(3);
    const unsigned ln = threadIdx.x & 63, d = (8u << sh) - 1024, k0 = ((((LEAD - jrot) & 7) - 1) >> 31) & (LEAD * 128);
    const unsigned vA0 = ((ln >> 3) << sh) + (((ln ^ (ln >> 3)) & 7) << 4), vA1 = vA0 + d, vA2 = vA1 + d, vA3 = vA2 + d;
    dma4<0>(lA0, vA0, vA1, vA2, vA3, sA0, oA0 + k0);
    dma4<0>(lA1, vA0, vA1, vA2, vA3, sA1, oA1 + k0);
  }
  asm volatile("" : "+s"(sA0), "+s"(sA1), "+s"(oA0), "+s"(oA1), "+s"(lA0), "+s"(lA1));
  char* const a2 = ws + WS_A2;
  const int npi = NPI + 2 * (~xg & 1);
  int NTR = isP ? 16 - NOQ : NOQ, so = isP ? NOQ : 0, T2 = isP ? 16 - NOQ + npi : NOQ + 32 - npi;
  asm volatile("" : "+s"(NTR), "+s"(so), "+s"(T2));
  auto jof = [&](int s) { return (((s >> 3) ^ hf) << 3) + ((s + tm) & 7); };
  const unsigned PART_A = ((0x1302 >> (4 * (T2 & 3))) & 3) * 16384, PART_B = 65536 + ((0x2031 >> (4 * (T2 & 3))) & 3) * 16384;
  const int own = isP ? -1 : (hp - tm) & 7;
  auto jpos = [&](int n) { const int r = n - NTR, a = so + (n > own ? n : n == 0 ? own : n - 1), c = r < 16 ? r : r - 16 + npi; return jof((n < NTR ? a : c) & 15); };

  const char* const mbox = ws + WS_MB + ((isP ? 0 : 128) + tm * 8 + tn) * 256;
  auto fcopy = [&](int ln) { asm volatile("s_mov_b32 m0, %0\n\ts_nop 0\n\tglobal_load_lds_dword %1, %2 sc1" ::"s"(L_POLL), "v"(ln * 4), "s"(mbox) : "memory"); };

  if (__builtin_expect(wid >= 8, 1)) {
    const int w = wid - 8;
    LANES;
    const unsigned r8 = lane >> 3, c8 = lane & 7;
    const unsigned swzB = (c8 ^ (((lane >> 4) & 1) << 1)) << 4, vL = lane * 16;
    const unsigned vA0 = (r8 << sh) + ((c8 ^ r8) << 4), vA1 = vA0 + (8u << sh) - 1024, vA2 = vA1 + (8u << sh) - 1024, vA3 = vA2 + (8u << sh) - 1024;
    const i32x4 sX = mksrd(x, 2048 * 2048);
    const int ktab = lead(lane, jrot, LEAD, isP ? 32 : 16);
    auto kof = [&](int t) { return __builtin_amdgcn_readlane(ktab, t); };
    const unsigned wS0 = (w & 1) * 32 * 2048 + (tn * 128 + (w >> 1) * 64) * 2;
    const unsigned vT0 = r8 * 2048 + swzB, vT1 = (vT0 ^ 64) + 15360, vT2 = vT0 + 2 * 15360, vT3 = vT1 + 2 * 15360, lPB = L_PB + (w >> 1) * 24576 + (w & 1) * 4096;
    auto issBp = [&](int t, unsigned sl) { dma4<0>(sl * 8192 + lPB, vT0, vT1, vT2, vT3, sX, wS0 + kof(t) * 64 * 2048); };
    auto issAp = [&](int t, unsigned sl) {
      dma4<0>(sl * 16384 + lA0, vA0, vA1, vA2, vA3, sA0, oA0 + kof(t) * 128);
      dma4<0>(sl * 16384 + lA1, vA0, vA1, vA2, vA3, sA1, oA1 + kof(t) * 128);
    };
    const i32x4 sW = mksrd(wqkv, 1024 * 6144);
    const unsigned vH = r8 * 2048 + ((c8 ^ ((4 * (w & 1) + r8) & 7)) << 4), vB0 = r8 * 6144 + swzB, vB1 = (vB0 ^ 64) + 8 * 6144 - 1024;
    const unsigned sH0 = ((pan ? pan * 256 - 16 : 0) + 4 * w) * 2048, sB0 = hp * 128 + w * 16 * 6144, lQB = L_QB + w * 2048;
    auto issB = [&](int t, unsigned sl) {
      const unsigned o = sB0 + kof(t) * 64 * 6144, l = lQB + sl * 24576;
      dma2(l, vB0, vB1, sW, o); dma2(l + 8192, vB0, vB1, sW, o + 2048); dma2(l + 16384, vB0, vB1, sW, o + 4096);
    };
    auto issH = [&](int t, unsigned sl) { dma_half(L_QH + sl * 2048 + w * 512, vH, sX, sH0 + kof(t) * 128); };
    auto issA = [&](int t, unsigned sl) {
      dma4<0>(sl * 8192 + lA0, vA0, vA1, vA2, vA3, sA0, oA0 + kof(t) * 128);
      dma4<0>(sl * 8192 + lA1, vA0, vA1, vA2, vA3, sA1, oA1 + kof(t) * 128);
      issH(t, sl);
    };
    if (isP) { issBp(0, 0); issBp(1, 1); } else { issB(0, 0); issH(0, 0); issB(1, 1); }
    int isP2 = so, isP3 = so;
    asm volatile("" : "+s"(isP2), "+s"(isP3));
    i32x4 sS = mksrd(w < 2 ? (const void*)a2 : (const void*)wo, w < 2 ? 12u << 20 : 1024 * 2048);
    const u16* ph = isP ? hi : hr;
    unsigned w8k = (w & 1) * 8192;
    unsigned lPt = (w < 2 ? PART_A : PART_B) + w8k, oPt = w * 8192;
    asm volatile("" : "+s"(lPt), "+s"(oPt), "+s"(w8k), "+s"(sS), "+s"(ph));
    const bool ltr = lane < NTR;
    const unsigned jl = jpos(lane), cB = tn * 128 + (w & 1) * 64;
    unsigned vW0 = r8 * 2048 + ((c8 ^ (r8 & 6)) << 4), vW1 = vW0 + 15360, vW2 = vW0 + 2 * 15360, vW3 = vW0 + 3 * 15360;
    unsigned vR0 = r8 * 2048 + ((c8 ^ r8) << 4), vR1 = vR0 + 15360, vR2 = vR0 + 2 * 15360, vR3 = vR0 + 3 * 15360;
    const unsigned oa = tm * 262144 + w8k + (ltr ? 2u << 22 : lane >= NTR + (isP ? 0 : 16) ? 1u << 22 : 0u) + jl * 16384, ob = ltr ? cB * 2 + jl * 64 * 2048 : cB * 2048 + jl * 128;
    unsigned tOff = w < 2 ? oa : ob, tLds = (w < 2 ? 0 : L_SB) + (((w < 2 ? 0x1302 : 0x2031) >> (4 * (lane & 3))) & 3) * 16384 + w8k, tFlag = ltr ? 8 * jl : 128 + (jl >> 1) * 8;
    const unsigned fsh = isP ? 3 : 4, fmb = WS_MB + ((lane >> fsh) * 128 + (isP ? tm * 8 : pan * 16) + (lane & ((1 << fsh) - 1))) * 256 + (isP ? 128 + tn * 8 : hp * 8);
    unsigned fo = (lane >> fsh) < 2 ? fmb : WS_ACK + b * 8;
    asm volatile("" : "+v"(vW0), "+v"(vW1), "+v"(vW2), "+v"(vW3), "+v"(vR0), "+v"(vR1), "+v"(vR2), "+v"(vR3), "+v"(tOff), "+v"(tLds), "+v"(tFlag), "+v"(fo));
    if (isP3) { WAITV(4); BAR(); issAp(1, 1); issBp(2, 2); issAp(2, 2); }
    else { WAITV(6); BAR(); issA(1, 1); issA(2, 2); }
    auto sB = [&](int n) { const unsigned l = __builtin_amdgcn_readlane(tLds, n), o = __builtin_amdgcn_readlane(tOff, n);
                           dma4n<0>(l, vW0, vW1, vW2, vW3, sS, o); dma4<0>(l + 4096, vW0, vW1, vW2, vW3, sS, o + 32 * 2048); };
    auto sBr = [&](int n, i32x4 sr) { const unsigned l = __builtin_amdgcn_readlane(tLds, n), o = __builtin_amdgcn_readlane(tOff, n);
                                      dma4n<0>(l, vR0, vR1, vR2, vR3, sr, o); dma4<0>(l + 4096, vR0, vR1, vR2, vR3, sr, o + 32 * 2048); };
    auto sA = [&](int n) { const unsigned l = __builtin_amdgcn_readlane(tLds, n), o = __builtin_amdgcn_readlane(tOff, n);
                           dma4n<2>(l, vL, vL, vL, vL, sS, o); dma4<2>(l + 4096, vL, vL, vL, vL, sS, o + 4096); };
    int next = 0, rdy = 0;
    u64 tag;
    auto epoch_ask = [&]() { asm volatile("global_load_dwordx2 %0, %1, %2 sc1" : "=v"(tag) : "v"(0u), "s"(ws + WS_EPOCH) : "memory"); };
    auto epoch_use = [&]() {
      asm volatile("" : "+v"(tag));
      tag = mktag(((u64)__builtin_amdgcn_readfirstlane((unsigned)(tag >> 32)) << 32) | (unsigned)__builtin_amdgcn_readfirstlane((unsigned)tag));
      *(LAS u64*)(size_t)L_TAG = tag;
    };
    gu64* const gp = (gu64*)(ws + WS_G) + (isP ? tm * 128 + (w & 1) * 64 : pan * 256 + w * 64) + lane;
    u64 gval;
    auto gate_ask = [&]() { asm volatile("global_load_dwordx2 %0, %1, off sc1" : "=v"(gval) : "v"(gp) : "memory"); };
    auto gate_put = [&]() {
      asm volatile("" : "+v"(gval));
      _Pragma("nounroll") for (int spin = 0; spin < (1 << 20) && !__all((unsigned)(gval >> 32) == (unsigned)tag); ++spin)
        asm volatile("global_load_dwordx2 %0, %1, off sc1\n\ts_waitcnt vmcnt(0)" : "=&v"(gval) : "v"(gp) : "memory");
      float sc = __uint_as_float((unsigned)gval);
      if (isP) { sc = rbf(1.0f - sc); if (w >= 2) sc = -sc; }
      *(LAS float*)(size_t)(L_G + (w * 64 + l15 * 4 + g) * 4) = sc;
    };
    const u64 stop = 1ull << T2;
    const u64 mine = isP ? 0ull : 1ull;
    auto count = [&](u64 v) { const int r = __builtin_ctzll(~(__ballot(v == tag) | mine) | stop); rdy = r > rdy ? r : rdy; };
    auto seen = [&]() { count(*(volatile LAS u64*)(size_t)(L_POLL + tFlag)); };
    auto poll = [&]() { count(__hip_atomic_load((gu64*)(mbox + tFlag), RLX_AGENT)); };
    if (isP2) {
      unsigned sl = 0;
      _Pragma("nounroll") for (int t = 0; t < 29; ++t) {
        WAITV(12); asm volatile("s_barrier" ::: "memory");
        issBp(t + 3, sl); issAp(t + 3, sl);
        sl = sl == 2 ? 0 : sl + 1;
      }
      gate_ask(); epoch_ask();
      WAITV(14); BAR();
      if (w < 2) { fcopy(lane); WAITV(1); } else { sB(0); WAITV(8); }
      epoch_use(); gate_put();
      BAR();
      if (w < 2) { fcopy(lane); seen(); _Pragma("nounroll") while (next < 2 && next < rdy) { sA(next); ++next; } } else sB(1);
      BAR();
      if (w < 2) {
        seen();
        const u64 t0 = __builtin_amdgcn_s_memtime();
        _Pragma("nounroll") for (;;) {
          _Pragma("nounroll") while (next < 2 && next < rdy) { sA(next); ++next; }
          if (next == 2 || __builtin_amdgcn_s_memtime() - t0 > PSPIN) break;
          poll();
        }
      }
    } else {
      const unsigned hA0 = L_QH + l15 * 128 + ((g ^ (lane & 7)) << 4);
      const unsigned hB0 = L_QB + (1 + (w & 1)) * 8192 + (8 * g + q) * 128 + (((2 * (w >> 1)) ^ f) << 5) + p * 8;
      f32x4 h0 = {0, 0, 0, 0}, h1 = {0, 0, 0, 0};
      auto halo = [&](unsigned sa, unsigned sb) {
        asm volatile(
            "ds_read_b128 v[100:103], %2\n\ts_nop 3\n\tds_read_b128 v[104:107], %3\n\ts_nop 3\n\t"
            "ds_read_b64_tr_b16 v[108:109], %4\n\ts_nop 3\n\tds_read_b64_tr_b16 v[110:111], %4 offset:512\n\ts_nop 3\n\tds_read_b64_tr_b16 v[112:113], %5\n\ts_nop 3\n\tds_read_b64_tr_b16 v[114:115], %5 offset:512\n\ts_nop 3\n\t"
            "ds_read_b64_tr_b16 v[116:117], %4 offset:4096\n\ts_nop 3\n\tds_read_b64_tr_b16 v[118:119], %4 offset:4608\n\ts_nop 3\n\tds_read_b64_tr_b16 v[120:121], %5 offset:4096\n\ts_nop 3\n\tds_read_b64_tr_b16 v[122:123], %5 offset:4608\n\ts_nop 3\n\t"
            "s_bitcmp1_b32 %6, 0\n\ts_cbranch_scc1 HV%=\n\ts_waitcnt lgkmcnt(0)\n\t"
            "v_mfma_f32_16x16x32_bf16 %0, v[108:111], v[100:103], %0\n\tv_mfma_f32_16x16x32_bf16 %0, v[116:119], v[104:107], %0\n\t"
            "v_mfma_f32_16x16x32_bf16 %1, v[120:123], v[104:107], %1\n\tv_mfma_f32_16x16x32_bf16 %1, v[112:115], v[100:103], %1\n\t"
            "s_branch HE%=\n\tHV%=:\n\ts_waitcnt lgkmcnt(0)\n\t"
            "v_mfma_f32_16x16x32_bf16 %0, v[100:103], v[108:111], %0\n\tv_mfma_f32_16x16x32_bf16 %0, v[104:107], v[116:119], %0\n\t"
            "v_mfma_f32_16x16x32_bf16 %1, v[104:107], v[120:123], %1\n\tv_mfma_f32_16x16x32_bf16 %1, v[100:103], v[112:115], %1\n\t"
            "HE%=:\n\ts_nop 7"
            : "+{v[84:87]}"(h0), "+{v[88:91]}"(h1)
            : "v"(hA0 + sa * 2048), "v"((hA0 ^ 64) + sa * 2048), "v"(hB0 + sb * 24576), "v"((hB0 ^ 32) + sb * 24576), "s"(w)
            : "memory", "scc", "v100", "v101", "v102", "v103", "v104", "v105", "v106", "v107", "v108", "v109", "v110", "v111", "v112", "v113", "v114", "v115", "v116", "v117", "v118", "v119", "v120", "v121", "v122", "v123");
      };
      halo(0, 0);
      unsigned sa = 0;
      auto steps = [&](int t0, int t1) {
        _Pragma("nounroll") for (int t = t0; t < t1; ++t) {
          WAITV(9); BAR();
          const unsigned sn = sa == 2 ? 0 : sa + 1;
          issB(t + 2, t & 1); halo(sn, (t + 1) & 1); issA(t + 3, sa);
          sa = sn;
        }
      };
      u32x4 gx[4], gg[2];
      const int grow = (b - 128) * 16 + w * 4;
      auto gate_load = [&](int r) {
        asm volatile("global_load_dwordx4 %0, %4, %5\n\tglobal_load_dwordx4 %1, %4, %5 offset:1024\n\tglobal_load_dwordx4 %2, %4, %5 offset:2048\n\tglobal_load_dwordx4 %3, %4, %5 offset:3072"
                     : "=&v"(gx[0]), "=&v"(gx[1]), "=&v"(gx[2]), "=&v"(gx[3]) : "v"(vL), "s"((const char*)x + (size_t)(grow + r) * 2048) : "memory");
      };
      auto gate_use = [&](int r) {
        asm volatile("" : "+v"(gx[0]), "+v"(gx[1]), "+v"(gx[2]), "+v"(gx[3]), "+v"(gg[0]), "+v"(gg[1]));
        const float gv = gate2(gx, gg);
        if ((lane & 31) == 0) __hip_atomic_store((gu64*)(ws + WS_G) + grow + r + (lane >> 5), (tag << 32) | __float_as_uint(gv), RLX_AGENT);
      };
      steps(0, 3);
      asm volatile("global_load_dwordx4 %0, %2, %3\n\tglobal_load_dwordx4 %1, %2, %3 offset:1024" : "=&v"(gg[0]), "=&v"(gg[1]) : "v"(vL), "s"(gw) : "memory");
      gate_load(0); epoch_ask();
      WAITV(16); BAR(); issB(5, 1); halo(1, 0); issA(6, 0);
      WAITV(9); BAR(); issB(6, 0); halo(2, 1); issA(7, 1);
      epoch_use(); gate_use(0); gate_load(2);
      WAITV(14); BAR(); issB(7, 1); halo(0, 0); issA(8, 2);
      WAITV(9); BAR(); issB(8, 0); halo(1, 1); issA(9, 0);
      gate_use(2);
      sa = 1;
      steps(7, 13);
      WAITV(9); BAR();
      gate_ask();
      issB(15, 1);
      halo(2, 0);
      WAITV(0);
      gate_put();
      BAR();
      if (w >= 2) { sB(0); sB(1); }
      halo(0, 1);
      BAR();
      asm volatile("" : "+v"(h0), "+v"(h1));
      const u32x4 hv = {cvtpk(h0[0], h0[1]), cvtpk(h0[2], h0[3]), cvtpk(h1[0], h1[1]), cvtpk(h1[2], h1[3])};
      *(LAS u32x4*)(size_t)(L_X + (w >> 1) * 10240 + (w & 1) * 1024 + lane * 16) = hv;
      BAR();
      if (w >= 2) { sB(2); sB(3); }
    }
    BAR();
    int t = T2 - 3;
    if (w < 2) {
      if (!isP) { sA(0); next = rdy = 1; }
      auto spin_until = [&](int need) {
        _Pragma("nounroll") for (int spin = 1 << 21; rdy <= need; --spin) { poll(); if (spin == 0) rdy = T2; }
      };
      int t4 = 3;
      if (next > 0) { WAITV(0); BAR(); t4 = 4; }
      _Pragma("nounroll") for (;;) {
        if (rdy < t4) seen();
        spin_until(t4 - 3);
        const int lim = rdy < t4 ? rdy : t4;
        _Pragma("nounroll") while (next < lim) { sA(next); ++next; }
        if (next >= t4) {
          WAITV(16); BAR(); ++t4;
          if (next + 1 == t4) { _Pragma("nounroll") while (next < rdy) { sA(next); ++next; WAITV(16); BAR(); } t4 = next + 1; }
        } else {
          if (next + 1 == t4) WAITV(8); else WAITV(0);
          BAR(); ++t4;
        }
        if (next == T2) break;
      }
      t = t4 - 4;
    } else {
      if (w == 2) __hip_atomic_store((gu64*)(ws + fo), tag, RLX_AGENT);
      if (w == 2) WAITV(1); else WAITV(0);
      BAR();
      if (isP) { sB(2); sB(3); }
      int n = 4;
      _Pragma("nounroll") for (; n < NTR; ++n) { WAITV(16); BAR(); if (w == 2) fcopy(lane); sB(n); }
      const int n2 = isP ? T2 : NTR + 16;
      _Pragma("nounroll") for (; n < n2; ++n) { WAITV(16); BAR(); if (w == 2) fcopy(lane); sBr(n, mksrd(ph, 1024 * 2048)); }
      _Pragma("nounroll") for (; n < T2; ++n) { WAITV(16); BAR(); if (w == 2) fcopy(lane); sBr(n, mksrd(hi, 1024 * 2048)); }
      WAITV(16); BAR();
    }
    bool got = false;
    auto get_part = [&]() {
      const i32x4 sPt = mksrd(ws + WS_PART + (size_t)(tm * 8 + tn) * 32768, 32768);
      dma4<2>(lPt, vL, vL, vL, vL, sPt, oPt); dma4<2>(lPt + 4096, vL, vL, vL, vL, sPt, oPt + 4096);
      got = true;
    };
    if (isP) {
      if (t == T2 - 3) { WAITV(8); BAR(); ++t; }
      WAITV(0);
      if (t == T2 - 2) { BAR(); }
      BAR();
      if (b == 0 && w == 0) {
        gu64* const ak = (gu64*)(ws + WS_ACK) + lane;
        _Pragma("nounroll") for (int spin = 0; spin < (1 << 20); ++spin) {
          const u64 a0 = __hip_atomic_load(ak, RLX_AGENT), a1 = __hip_atomic_load(ak + 64, RLX_AGENT), a2_ = __hip_atomic_load(ak + 128, RLX_AGENT), a3 = __hip_atomic_load(ak + 192, RLX_AGENT);
          if (__all(a0 == tag && a1 == tag && a2_ == tag && a3 == tag)) break;
        }
        if (lane == 0) __hip_atomic_store((gu64*)(ws + WS_EPOCH), tag, RLX_AGENT);
      }
    } else {
      auto look = [&]() { fcopy(lane); if (!got && __all(*(volatile LAS u64*)(size_t)(L_POLL + 192 + w * 8) == tag)) get_part(); };
      if (t == T2 - 3) { look(); if (got) WAITV(17); else WAITV(9); BAR(); look(); if (got) WAITV(10); else WAITV(2); BAR(); }
      else if (t == T2 - 2) { look(); if (got) WAITV(9); else WAITV(1); BAR(); }
      if (!got) {
        _Pragma("nounroll") for (int spin = 0; spin < (1 << 21); ++spin) if (__all(__hip_atomic_load((gu64*)(mbox + 192 + w * 8), RLX_AGENT) == tag)) break;
        get_part();
      }
      WAITV(0);
      BAR();
    }
  } else {
    const int wm = wid >> 1, wn = wid & 1;
    if (wid == 3) { LANES; fcopy(lane); }
    if (isP) {
      LANES;
      const unsigned a0 = wm * 49152 + l15 * 128 + ((g ^ (lane & 7)) << 4);
      const unsigned b0 = L_PB + wn * 24576 + (8 * g + q) * 128 + (f << 5) + p * 8;
      const unsigned ga = L_G + (wm * 128 + l15 * 4) * 4, vo = ((((g & 1) << 1) | (g >> 1)) * 16 + l15) * 16;
      const i32x4 sO = mksrd(a2, 12u << 20);
      const unsigned so0 = (wm << 22) + tm * 262144 + (tn * 2 + wn) * 16384;
      asm volatile(
  "v_mov_b64 v[0:1], 0\n\t"
  "v_mov_b64 v[2:3], 0\n\t"
  "v_mov_b64 v[4:5], 0\n\t"
  "v_mov_b64 v[6:7], 0\n\t"
  "v_mov_b64 v[8:9], 0\n\t"
  "v_mov_b64 v[10:11], 0\n\t"
  "v_mov_b64 v[12:13], 0\n\t"
  "v_mov_b64 v[14:15], 0\n\t"
  "v_mov_b64 v[16:17], 0\n\t"
  "v_mov_b64 v[18:19], 0\n\t"
  "v_mov_b64 v[20:21], 0\n\t"
  "v_mov_b64 v[22:23], 0\n\t"
  "v_mov_b64 v[24:25], 0\n\t"
  "v_mov_b64 v[26:27], 0\n\t"
  "v_mov_b64 v[28:29], 0\n\t"
  "v_mov_b64 v[30:31], 0\n\t"
  "v_mov_b64 v[32:33], 0\n\t"
  "v_mov_b64 v[34:35], 0\n\t"
  "v_mov_b64 v[36:37], 0\n\t"
  "v_mov_b64 v[38:39], 0\n\t"
  "v_mov_b64 v[40:41], 0\n\t"
  "v_mov_b64 v[42:43], 0\n\t"
  "v_mov_b64 v[44:45], 0\n\t"
  "v_mov_b64 v[46:47], 0\n\t"
  "v_mov_b64 v[48:49], 0\n\t"
  "v_mov_b64 v[50:51], 0\n\t"
  "v_mov_b64 v[52:53], 0\n\t"
  "v_mov_b64 v[54:55], 0\n\t"
  "v_mov_b64 v[56:57], 0\n\t"
  "v_mov_b64 v[58:59], 0\n\t"
  "v_mov_b64 v[60:61], 0\n\t"
  "v_mov_b64 v[62:63], 0\n\t"
  "v_mov_b64 v[64:65], 0\n\t"
  "v_mov_b64 v[66:67], 0\n\t"
  "v_mov_b64 v[68:69], 0\n\t"
  "v_mov_b64 v[70:71], 0\n\t"
  "v_mov_b64 v[72:73], 0\n\t"
  "v_mov_b64 v[74:75], 0\n\t"
  "v_mov_b64 v[76:77], 0\n\t"
  "v_mov_b64 v[78:79], 0\n\t"
  "v_mov_b64 v[80:81], 0\n\t"
  "v_mov_b64 v[82:83], 0\n\t"
  "v_mov_b64 v[84:85], 0\n\t"
  "v_mov_b64 v[86:87], 0\n\t"
  "v_mov_b64 v[88:89], 0\n\t"
  "v_mov_b64 v[90:91], 0\n\t"
  "v_mov_b64 v[92:93], 0\n\t"
  "v_mov_b64 v[94:95], 0\n\t"
  "v_mov_b64 v[96:97], 0\n\t"
  "v_mov_b64 v[98:99], 0\n\t"
  "v_mov_b64 v[100:101], 0\n\t"
  "v_mov_b64 v[102:103], 0\n\t"
  "v_mov_b64 v[104:105], 0\n\t"
  "v_mov_b64 v[106:107], 0\n\t"
  "v_mov_b64 v[108:109], 0\n\t"
  "v_mov_b64 v[110:111], 0\n\t"
  "v_mov_b64 v[112:113], 0\n\t"
  "v_mov_b64 v[114:115], 0\n\t"
  "v_mov_b64 v[116:117], 0\n\t"
  "v_mov_b64 v[118:119], 0\n\t"
  "v_mov_b64 v[120:121], 0\n\t"
  "v_mov_b64 v[122:123], 0\n\t"
  "v_mov_b64 v[124:125], 0\n\t"
  "v_mov_b64 v[126:127], 0\n\t"
  "s_barrier\n\t"
  "ds_read_b128 v[160:163], v192 offset:0\n\t"
  "ds_read_b128 v[164:167], v193 offset:0\n\t"
  "ds_read_b128 v[168:171], v192 offset:2048\n\t"
  "ds_read_b128 v[172:175], v193 offset:2048\n\t"
  "ds_read_b64_tr_b16 v[128:129], v194 offset:0\n\t"
  "ds_read_b64_tr_b16 v[130:131], v194 offset:512\n\t"
  "ds_read_b64_tr_b16 v[132:133], v194 offset:4096\n\t"
  "ds_read_b64_tr_b16 v[134:135], v194 offset:4608\n\t"
  "ds_read_b64_tr_b16 v[136:137], v195 offset:0\n\t"
  "ds_read_b64_tr_b16 v[138:139], v195 offset:512\n\t"
  "ds_read_b64_tr_b16 v[140:141], v195 offset:4096\n\t"
  "ds_read_b64_tr_b16 v[142:143], v195 offset:4608\n\t"
  "ds_read_b64_tr_b16 v[144:145], v196 offset:0\n\t"
  "ds_read_b64_tr_b16 v[146:147], v196 offset:512\n\t"
  "ds_read_b64_tr_b16 v[148:149], v196 offset:4096\n\t"
  "ds_read_b64_tr_b16 v[150:151], v196 offset:4608\n\t"
  "s_waitcnt lgkmcnt(10)\n\t"
  "v_mfma_f32_16x16x32_bf16 v[0:3], v[128:131], v[160:163], v[0:3]\n\t"
  "ds_read_b64_tr_b16 v[152:153], v197 offset:0\n\t"
  "s_waitcnt lgkmcnt(9)\n\t"
  "v_mfma_f32_16x16x32_bf16 v[0:3], v[132:135], v[164:167], v[0:3]\n\t"
  "ds_read_b64_tr_b16 v[154:155], v197 offset:512\n\t"
  "v_mfma_f32_16x16x32_bf16 v[16:19], v[132:135], v[172:175], v[16:19]\n\t"
  "ds_read_b64_tr_b16 v[156:157], v197 offset:4096\n\t"
  "v_mfma_f32_16x16x32_bf16 v[16:19], v[128:131], v[168:171], v[16:19]\n\t"
  "ds_read_b64_tr_b16 v[158:159], v197 offset:4608\n\t"
  "s_waitcnt lgkmcnt(10)\n\t"
  "v_mfma_f32_16x16x32_bf16 v[20:23], v[136:139], v[168:171], v[20:23]\n\t"
  "ds_read_b128 v[176:179], v192 offset:4096\n\t"
  "s_waitcnt lgkmcnt(9)\n\t"
  "v_mfma_f32_16x16x32_bf16 v[20:23], v[140:143], v[172:175], v[20:23]\n\t"
  "ds_read_b128 v[180:183], v193 offset:4096\n\t"
  "v_mfma_f32_16x16x32_bf16 v[4:7], v[140:143], v[164:167], v[4:7]\n\t"
  "ds_read_b128 v[184:187], v192 offset:6144\n\t"
  "v_mfma_f32_16x16x32_bf16 v[4:7], v[136:139], v[160:163], v[4:7]\n\t"
  "ds_read_b128 v[188:191], v193 offset:6144\n\t"
  "s_waitcnt lgkmcnt(8)\n\t"
  "v_mfma_f32_16x16x32_bf16 v[8:11], v[144:147], v[160:163], v[8:11]\n\t"
  "v_mfma_f32_16x16x32_bf16 v[8:11], v[148:151], v[164:167], v[8:11]\n\t"
  "v_mfma_f32_16x16x32_bf16 v[24:27], v[148:151], v[172:175], v[24:27]\n\t"
  "v_mfma_f32_16x16x32_bf16 v[24:27], v[144:147], v[168:171], v[24:27]\n\t"
  "s_waitcnt lgkmcnt(4)\n\t"
  "v_mfma_f32_16x16x32_bf16 v[28:31], v[152:155], v[168:171], v[28:31]\n\t"
  "v_mfma_f32_16x16x32_bf16 v[28:31], v[156:159], v[172:175], v[28:31]\n\t"
  "v_mfma_f32_16x16x32_bf16 v[12:15], v[156:159], v[164:167], v[12:15]\n\t"
  "v_mfma_f32_16x16x32_bf16 v[12:15], v[152:155], v[160:163], v[12:15]\n\t"
  "s_waitcnt lgkmcnt(3)\n\t"
  "v_mfma_f32_16x16x32_bf16 v[32:35], v[128:131], v[176:179], v[32:35]\n\t"
  "ds_read_b128 v[160:163], v192 offset:8192\n\t"
  "s_waitcnt lgkmcnt(3)\n\t"
  "v_mfma_f32_16x16x32_bf16 v[32:35], v[132:135], v[180:183], v[32:35]\n\t"
  "ds_read_b128 v[164:167], v193 offset:8192\n\t"
  "s_waitcnt lgkmcnt(2)\n\t"
  "v_mfma_f32_16x16x32_bf16 v[48:51], v[132:135], v[188:191], v[48:51]\n\t"
  "ds_read_b128 v[168:171], v192 offset:10240\n\t"
  "v_mfma_f32_16x16x32_bf16 v[48:51], v[128:131], v[184:187], v[48:51]\n\t"
  "ds_read_b128 v[172:175], v193 offset:10240\n\t"
  "v_mfma_f32_16x16x32_bf16 v[52:55], v[136:139], v[184:187], v[52:55]\n\t"
  "v_mfma_f32_16x16x32_bf16 v[52:55], v[140:143], v[188:191], v[52:55]\n\t"
  "v_mfma_f32_16x16x32_bf16 v[36:39], v[140:143], v[180:183], v[36:39]\n\t"
  "v_mfma_f32_16x16x32_bf16 v[36:39], v[136:139], v[176:179], v[36:39]\n\t"
  "v_mfma_f32_16x16x32_bf16 v[40:43], v[144:147], v[176:179], v[40:43]\n\t"
  "v_mfma_f32_16x16x32_bf16 v[40:43], v[148:151], v[180:183], v[40:43]\n\t"
  "v_mfma_f32_16x16x32_bf16 v[56:59], v[148:151], v[188:191], v[56:59]\n\t"
  "v_mfma_f32_16x16x32_bf16 v[56:59], v[144:147], v[184:187], v[56:59]\n\t"
  "v_mfma_f32_16x16x32_bf16 v[60:63], v[152:155], v[184:187], v[60:63]\n\t"
  "v_mfma_f32_16x16x32_bf16 v[60:63], v[156:159], v[188:191], v[60:63]\n\t"
  "v_mfma_f32_16x16x32_bf16 v[44:47], v[156:159], v[180:183], v[44:47]\n\t"
  "v_mfma_f32_16x16x32_bf16 v[44:47], v[152:155], v[176:179], v[44:47]\n\t"
  "s_waitcnt lgkmcnt(3)\n\t"
  "v_mfma_f32_16x16x32_bf16 v[64:67], v[128:131], v[160:163], v[64:67]\n\t"
  "ds_read_b128 v[176:179], v192 offset:12288\n\t"
  "s_waitcnt lgkmcnt(3)\n\t"
  "v_mfma_f32_16x16x32_bf16 v[64:67], v[132:135], v[164:167], v[64:67]\n\t"
  "ds_read_b128 v[180:183], v193 offset:12288\n\t"
  "s_waitcnt lgkmcnt(2)\n\t"
  "v_mfma_f32_16x16x32_bf16 v[80:83], v[132:135], v[172:175], v[80:83]\n\t"
  "ds_read_b128 v[184:187], v192 offset:14336\n\t"
  "v_mfma_f32_16x16x32_bf16 v[80:83], v[128:131], v[168:171], v[80:83]\n\t"
  "ds_read_b128 v[188:191], v193 offset:14336\n\t"
  "v_mfma_f32_16x16x32_bf16 v[84:87], v[136:139], v[168:171], v[84:87]\n\t"
  "v_mfma_f32_16x16x32_bf16 v[84:87], v[140:143], v[172:175], v[84:87]\n\t"
  "v_mfma_f32_16x16x32_bf16 v[68:71], v[140:143], v[164:167], v[68:71]\n\t"
  "v_mfma_f32_16x16x32_bf16 v[68:71], v[136:139], v[160:163], v[68:71]\n\t"
  "v_mfma_f32_16x16x32_bf16 v[72:75], v[144:147], v[160:163], v[72:75]\n\t"
  "v_mfma_f32_16x16x32_bf16 v[72:75], v[148:151], v[164:167], v[72:75]\n\t"
  "v_mfma_f32_16x16x32_bf16 v[88:91], v[148:151], v[172:175], v[88:91]\n\t"
  "v_mfma_f32_16x16x32_bf16 v[88:91], v[144:147], v[168:171], v[88:91]\n\t"
  "v_mfma_f32_16x16x32_bf16 v[92:95], v[152:155], v[168:171], v[92:95]\n\t"
  "v_mfma_f32_16x16x32_bf16 v[92:95], v[156:159], v[172:175], v[92:95]\n\t"
  "v_mfma_f32_16x16x32_bf16 v[76:79], v[156:159], v[164:167], v[76:79]\n\t"
  "v_mfma_f32_16x16x32_bf16 v[76:79], v[152:155], v[160:163], v[76:79]\n\t"
  "s_waitcnt lgkmcnt(0)\n\t"
  "s_mov_b32 s40, 10\n\t"
  ".p2align 6\n\t"
  "LPP%=:\n\t"
  "s_barrier\n\t"
  "v_mfma_f32_16x16x32_bf16 v[96:99], v[128:131], v[176:179], v[96:99]\n\t"
  "ds_read_b128 v[160:163], v192 offset:16384\n\t"
  "v_mfma_f32_16x16x32_bf16 v[96:99], v[132:135], v[180:183], v[96:99]\n\t"
  "ds_read_b128 v[164:167], v193 offset:16384\n\t"
  "v_mfma_f32_16x16x32_bf16 v[112:115], v[132:135], v[188:191], v[112:115]\n\t"
  "ds_read_b128 v[168:171], v192 offset:18432\n\t"
  "v_mfma_f32_16x16x32_bf16 v[112:115], v[128:131], v[184:187], v[112:115]\n\t"
  "ds_read_b128 v[172:175], v193 offset:18432\n\t"
  "v_mfma_f32_16x16x32_bf16 v[116:119], v[136:139], v[184:187], v[116:119]\n\t"
  "ds_read_b64_tr_b16 v[128:129], v194 offset:8192\n\t"
  "v_mfma_f32_16x16x32_bf16 v[116:119], v[140:143], v[188:191], v[116:119]\n\t"
  "ds_read_b64_tr_b16 v[130:131], v194 offset:8704\n\t"
  "v_mfma_f32_16x16x32_bf16 v[100:103], v[140:143], v[180:183], v[100:103]\n\t"
  "ds_read_b64_tr_b16 v[132:133], v194 offset:12288\n\t"
  "v_mfma_f32_16x16x32_bf16 v[100:103], v[136:139], v[176:179], v[100:103]\n\t"
  "ds_read_b64_tr_b16 v[134:135], v194 offset:12800\n\t"
  "v_mfma_f32_16x16x32_bf16 v[104:107], v[144:147], v[176:179], v[104:107]\n\t"
  "ds_read_b64_tr_b16 v[136:137], v195 offset:8192\n\t"
  "v_mfma_f32_16x16x32_bf16 v[104:107], v[148:151], v[180:183], v[104:107]\n\t"
  "ds_read_b64_tr_b16 v[138:139], v195 offset:8704\n\t"
  "v_mfma_f32_16x16x32_bf16 v[120:123], v[148:151], v[188:191], v[120:123]\n\t"
  "ds_read_b64_tr_b16 v[140:141], v195 offset:12288\n\t"
  "v_mfma_f32_16x16x32_bf16 v[120:123], v[144:147], v[184:187], v[120:123]\n\t"
  "ds_read_b64_tr_b16 v[142:143], v195 offset:12800\n\t"
  "v_mfma_f32_16x16x32_bf16 v[124:127], v[152:155], v[184:187], v[124:127]\n\t"
  "ds_read_b64_tr_b16 v[144:145], v196 offset:8192\n\t"
  "v_mfma_f32_16x16x32_bf16 v[124:127], v[156:159], v[188:191], v[124:127]\n\t"
  "ds_read_b64_tr_b16 v[146:147], v196 offset:8704\n\t"
  "v_mfma_f32_16x16x32_bf16 v[108:111], v[156:159], v[180:183], v[108:111]\n\t"
  "ds_read_b64_tr_b16 v[148:149], v196 offset:12288\n\t"
  "v_mfma_f32_16x16x32_bf16 v[108:111], v[152:155], v[176:179], v[108:111]\n\t"
  "ds_read_b64_tr_b16 v[150:151], v196 offset:12800\n\t"
  "s_waitcnt lgkmcnt(10)\n\t"
  "v_mfma_f32_16x16x32_bf16 v[0:3], v[128:131], v[160:163], v[0:3]\n\t"
  "ds_read_b64_tr_b16 v[152:153], v197 offset:8192\n\t"
  "s_waitcnt lgkmcnt(9)\n\t"
  "v_mfma_f32_16x16x32_bf16 v[0:3], v[132:135], v[164:167], v[0:3]\n\t"
  "ds_read_b64_tr_b16 v[154:155], v197 offset:8704\n\t"
  "v_mfma_f32_16x16x32_bf16 v[16:19], v[132:135], v[172:175], v[16:19]\n\t"
  "ds_read_b64_tr_b16 v[156:157], v197 offset:12288\n\t"
  "v_mfma_f32_16x16x32_bf16 v[16:19], v[128:131], v[168:171], v[16:19]\n\t"
  "ds_read_b64_tr_b16 v[158:159], v197 offset:12800\n\t"
  "s_waitcnt lgkmcnt(10)\n\t"
  "v_mfma_f32_16x16x32_bf16 v[20:23], v[136:139], v[168:171], v[20:23]\n\t"
  "ds_read_b128 v[176:179], v192 offset:20480\n\t"
  "s_waitcnt lgkmcnt(9)\n\t"
  "v_mfma_f32_16x16x32_bf16 v[20:23], v[140:143], v[172:175], v[20:23]\n\t"
  "ds_read_b128 v[180:183], v193 offset:20480\n\t"
  "v_mfma_f32_16x16x32_bf16 v[4:7], v[140:143], v[164:167], v[4:7]\n\t"
  "ds_read_b128 v[184:187], v192 offset:22528\n\t"
  "v_mfma_f32_16x16x32_bf16 v[4:7], v[136:139], v[160:163], v[4:7]\n\t"
  "ds_read_b128 v[188:191], v193 offset:22528\n\t"
  "s_waitcnt lgkmcnt(8)\n\t"
  "v_mfma_f32_16x16x32_bf16 v[8:11], v[144:147], v[160:163], v[8:11]\n\t"
  "v_mfma_f32_16x16x32_bf16 v[8:11], v[148:151], v[164:167], v[8:11]\n\t"
  "v_mfma_f32_16x16x32_bf16 v[24:27], v[148:151], v[172:175], v[24:27]\n\t"
  "v_mfma_f32_16x16x32_bf16 v[24:27], v[144:147], v[168:171], v[24:27]\n\t"
  "s_waitcnt lgkmcnt(4)\n\t"
  "v_mfma_f32_16x16x32_bf16 v[28:31], v[152:155], v[168:171], v[28:31]\n\t"
  "v_mfma_f32_16x16x32_bf16 v[28:31], v[156:159], v[172:175], v[28:31]\n\t"
  "v_mfma_f32_16x16x32_bf16 v[12:15], v[156:159], v[164:167], v[12:15]\n\t"
  "v_mfma_f32_16x16x32_bf16 v[12:15], v[152:155], v[160:163], v[12:15]\n\t"
  "s_waitcnt lgkmcnt(3)\n\t"
  "v_mfma_f32_16x16x32_bf16 v[32:35], v[128:131], v[176:179], v[32:35]\n\t"
  "ds_read_b128 v[160:163], v192 offset:24576\n\t"
  "s_waitcnt lgkmcnt(3)\n\t"
  "v_mfma_f32_16x16x32_bf16 v[32:35], v[132:135], v[180:183], v[32:35]\n\t"
  "ds_read_b128 v[164:167], v193 offset:24576\n\t"
  "s_waitcnt lgkmcnt(2)\n\t"
  "v_mfma_f32_16x16x32_bf16 v[48:51], v[132:135], v[188:191], v[48:51]\n\t"
  "ds_read_b128 v[168:171], v192 offset:26624\n\t"
  "v_mfma_f32_16x16x32_bf16 v[48:51], v[128:131], v[184:187], v[48:51]\n\t"
  "ds_read_b128 v[172:175], v193 offset:26624\n\t"
  "v_mfma_f32_16x16x32_bf16 v[52:55], v[136:139], v[184:187], v[52:55]\n\t"
  "v_mfma_f32_16x16x32_bf16 v[52:55], v[140:143], v[188:191], v[52:55]\n\t"
  "v_mfma_f32_16x16x32_bf16 v[36:39], v[140:143], v[180:183], v[36:39]\n\t"
  "v_mfma_f32_16x16x32_bf16 v[36:39], v[136:139], v[176:179], v[36:39]\n\t"
  "v_mfma_f32_16x16x32_bf16 v[40:43], v[144:147], v[176:179], v[40:43]\n\t"
  "v_mfma_f32_16x16x32_bf16 v[40:43], v[148:151], v[180:183], v[40:43]\n\t"
  "v_mfma_f32_16x16x32_bf16 v[56:59], v[148:151], v[188:191], v[56:59]\n\t"
  "v_mfma_f32_16x16x32_bf16 v[56:59], v[144:147], v[184:187], v[56:59]\n\t"
  "v_mfma_f32_16x16x32_bf16 v[60:63], v[152:155], v[184:187], v[60:63]\n\t"
  "v_mfma_f32_16x16x32_bf16 v[60:63], v[156:159], v[188:191], v[60:63]\n\t"
  "v_mfma_f32_16x16x32_bf16 v[44:47], v[156:159], v[180:183], v[44:47]\n\t"
  "v_mfma_f32_16x16x32_bf16 v[44:47], v[152:155], v[176:179], v[44:47]\n\t"
  "s_waitcnt lgkmcnt(3)\n\t"
  "v_mfma_f32_16x16x32_bf16 v[64:67], v[128:131], v[160:163], v[64:67]\n\t"
  "ds_read_b128 v[176:179], v192 offset:28672\n\t"
  "s_waitcnt lgkmcnt(3)\n\t"
  "v_mfma_f32_16x16x32_bf16 v[64:67], v[132:135], v[164:167], v[64:67]\n\t"
  "ds_read_b128 v[180:183], v193 offset:28672\n\t"
  "s_waitcnt lgkmcnt(2)\n\t"
  "v_mfma_f32_16x16x32_bf16 v[80:83], v[132:135], v[172:175], v[80:83]\n\t"
  "ds_read_b128 v[184:187], v192 offset:30720\n\t"
  "v_mfma_f32_16x16x32_bf16 v[80:83], v[128:131], v[168:171], v[80:83]\n\t"
  "ds_read_b128 v[188:191], v193 offset:30720\n\t"
  "v_mfma_f32_16x16x32_bf16 v[84:87], v[136:139], v[168:171], v[84:87]\n\t"
  "v_mfma_f32_16x16x32_bf16 v[84:87], v[140:143], v[172:175], v[84:87]\n\t"
  "v_mfma_f32_16x16x32_bf16 v[68:71], v[140:143], v[164:167], v[68:71]\n\t"
  "v_mfma_f32_16x16x32_bf16 v[68:71], v[136:139], v[160:163], v[68:71]\n\t"
  "v_mfma_f32_16x16x32_bf16 v[72:75], v[144:147], v[160:163], v[72:75]\n\t"
  "v_mfma_f32_16x16x32_bf16 v[72:75], v[148:151], v[164:167], v[72:75]\n\t"
  "v_mfma_f32_16x16x32_bf16 v[88:91], v[148:151], v[172:175], v[88:91]\n\t"
  "v_mfma_f32_16x16x32_bf16 v[88:91], v[144:147], v[168:171], v[88:91]\n\t"
  "v_mfma_f32_16x16x32_bf16 v[92:95], v[152:155], v[168:171], v[92:95]\n\t"
  "v_mfma_f32_16x16x32_bf16 v[92:95], v[156:159], v[172:175], v[92:95]\n\t"
  "v_mfma_f32_16x16x32_bf16 v[76:79], v[156:159], v[164:167], v[76:79]\n\t"
  "v_mfma_f32_16x16x32_bf16 v[76:79], v[152:155], v[160:163], v[76:79]\n\t"
  "s_waitcnt lgkmcnt(0)\n\t"
  "s_barrier\n\t"
  "v_mfma_f32_16x16x32_bf16 v[96:99], v[128:131], v[176:179], v[96:99]\n\t"
  "ds_read_b128 v[160:163], v192 offset:32768\n\t"
  "v_mfma_f32_16x16x32_bf16 v[96:99], v[132:135], v[180:183], v[96:99]\n\t"
  "ds_read_b128 v[164:167], v193 offset:32768\n\t"
  "v_mfma_f32_16x16x32_bf16 v[112:115], v[132:135], v[188:191], v[112:115]\n\t"
  "ds_read_b128 v[168:171], v192 offset:34816\n\t"
  "v_mfma_f32_16x16x32_bf16 v[112:115], v[128:131], v[184:187], v[112:115]\n\t"
  "ds_read_b128 v[172:175], v193 offset:34816\n\t"
  "v_mfma_f32_16x16x32_bf16 v[116:119], v[136:139], v[184:187], v[116:119]\n\t"
  "ds_read_b64_tr_b16 v[128:129], v194 offset:16384\n\t"
  "v_mfma_f32_16x16x32_bf16 v[116:119], v[140:143], v[188:191], v[116:119]\n\t"
  "ds_read_b64_tr_b16 v[130:131], v194 offset:16896\n\t"
  "v_mfma_f32_16x16x32_bf16 v[100:103], v[140:143], v[180:183], v[100:103]\n\t"
  "ds_read_b64_tr_b16 v[132:133], v194 offset:20480\n\t"
  "v_mfma_f32_16x16x32_bf16 v[100:103], v[136:139], v[176:179], v[100:103]\n\t"
  "ds_read_b64_tr_b16 v[134:135], v194 offset:20992\n\t"
  "v_mfma_f32_16x16x32_bf16 v[104:107], v[144:147], v[176:179], v[104:107]\n\t"
  "ds_read_b64_tr_b16 v[136:137], v195 offset:16384\n\t"
  "v_mfma_f32_16x16x32_bf16 v[104:107], v[148:151], v[180:183], v[104:107]\n\t"
  "ds_read_b64_tr_b16 v[138:139], v195 offset:16896\n\t"
  "v_mfma_f32_16x16x32_bf16 v[120:123], v[148:151], v[188:191], v[120:123]\n\t"
  "ds_read_b64_tr_b16 v[140:141], v195 offset:20480\n\t"
  "v_mfma_f32_16x16x32_bf16 v[120:123], v[144:147], v[184:187], v[120:123]\n\t"
  "ds_read_b64_tr_b16 v[142:143], v195 offset:20992\n\t"
  "v_mfma_f32_16x16x32_bf16 v[124:127], v[152:155], v[184:187], v[124:127]\n\t"
  "ds_read_b64_tr_b16 v[144:145], v196 offset:16384\n\t"
  "v_mfma_f32_16x16x32_bf16 v[124:127], v[156:159], v[188:191], v[124:127]\n\t"
  "ds_read_b64_tr_b16 v[146:147], v196 offset:16896\n\t"
  "v_mfma_f32_16x16x32_bf16 v[108:111], v[156:159], v[180:183], v[108:111]\n\t"
  "ds_read_b64_tr_b16 v[148:149], v196 offset:20480\n\t"
  "v_mfma_f32_16x16x32_bf16 v[108:111], v[152:155], v[176:179], v[108:111]\n\t"
  "ds_read_b64_tr_b16 v[150:151], v196 offset:20992\n\t"
  "s_waitcnt lgkmcnt(10)\n\t"
  "v_mfma_f32_16x16x32_bf16 v[0:3], v[128:131], v[160:163], v[0:3]\n\t"
  "ds_read_b64_tr_b16 v[152:153], v197 offset:16384\n\t"
  "s_waitcnt lgkmcnt(9)\n\t"
  "v_mfma_f32_16x16x32_bf16 v[0:3], v[132:135], v[164:167], v[0:3]\n\t"
  "ds_read_b64_tr_b16 v[154:155], v197 offset:16896\n\t"
  "v_mfma_f32_16x16x32_bf16 v[16:19], v[132:135], v[172:175], v[16:19]\n\t"
  "ds_read_b64_tr_b16 v[156:157], v197 offset:20480\n\t"
  "v_mfma_f32_16x16x32_bf16 v[16:19], v[128:131], v[168:171], v[16:19]\n\t"
  "ds_read_b64_tr_b16 v[158:159], v197 offset:20992\n\t"
  "s_waitcnt lgkmcnt(10)\n\t"
  "v_mfma_f32_16x16x32_bf16 v[20:23], v[136:139], v[168:171], v[20:23]\n\t"
  "ds_read_b128 v[176:179], v192 offset:36864\n\t"
  "s_waitcnt lgkmcnt(9)\n\t"
  "v_mfma_f32_16x16x32_bf16 v[20:23], v[140:143], v[172:175], v[20:23]\n\t"
  "ds_read_b128 v[180:183], v193 offset:36864\n\t"
  "v_mfma_f32_16x16x32_bf16 v[4:7], v[140:143], v[164:167], v[4:7]\n\t"
  "ds_read_b128 v[184:187], v192 offset:38912\n\t"
  "v_mfma_f32_16x16x32_bf16 v[4:7], v[136:139], v[160:163], v[4:7]\n\t"
  "ds_read_b128 v[188:191], v193 offset:38912\n\t"
  "s_waitcnt lgkmcnt(8)\n\t"
  "v_mfma_f32_16x16x32_bf16 v[8:11], v[144:147], v[160:163], v[8:11]\n\t"
  "v_mfma_f32_16x16x32_bf16 v[8:11], v[148:151], v[164:167], v[8:11]\n\t"
  "v_mfma_f32_16x16x32_bf16 v[24:27], v[148:151], v[172:175], v[24:27]\n\t"
  "v_mfma_f32_16x16x32_bf16 v[24:27], v[144:147], v[168:171], v[24:27]\n\t"
  "s_waitcnt lgkmcnt(4)\n\t"
  "v_mfma_f32_16x16x32_bf16 v[28:31], v[152:155], v[168:171], v[28:31]\n\t"
  "v_mfma_f32_16x16x32_bf16 v[28:31], v[156:159], v[172:175], v[28:31]\n\t"
  "v_mfma_f32_16x16x32_bf16 v[12:15], v[156:159], v[164:167], v[12:15]\n\t"
  "v_mfma_f32_16x16x32_bf16 v[12:15], v[152:155], v[160:163], v[12:15]\n\t"
  "s_waitcnt lgkmcnt(3)\n\t"
  "v_mfma_f32_16x16x32_bf16 v[32:35], v[128:131], v[176:179], v[32:35]\n\t"
  "ds_read_b128 v[160:163], v192 offset:40960\n\t"
  "s_waitcnt lgkmcnt(3)\n\t"
  "v_mfma_f32_16x16x32_bf16 v[32:35], v[132:135], v[180:183], v[32:35]\n\t"
  "ds_read_b128 v[164:167], v193 offset:40960\n\t"
  "s_waitcnt lgkmcnt(2)\n\t"
  "v_mfma_f32_16x16x32_bf16 v[48:51], v[132:135], v[188:191], v[48:51]\n\t"
  "ds_read_b128 v[168:171], v192 offset:43008\n\t"
  "v_mfma_f32_16x16x32_bf16 v[48:51], v[128:131], v[184:187], v[48:51]\n\t"
  "ds_read_b128 v[172:175], v193 offset:43008\n\t"
  "v_mfma_f32_16x16x32_bf16 v[52:55], v[136:139], v[184:187], v[52:55]\n\t"
  "v_mfma_f32_16x16x32_bf16 v[52:55], v[140:143], v[188:191], v[52:55]\n\t"
  "v_mfma_f32_16x16x32_bf16 v[36:39], v[140:143], v[180:183], v[36:39]\n\t"
  "v_mfma_f32_16x16x32_bf16 v[36:39], v[136:139], v[176:179], v[36:39]\n\t"
  "v_mfma_f32_16x16x32_bf16 v[40:43], v[144:147], v[176:179], v[40:43]\n\t"
  "v_mfma_f32_16x16x32_bf16 v[40:43], v[148:151], v[180:183], v[40:43]\n\t"
  "v_mfma_f32_16x16x32_bf16 v[56:59], v[148:151], v[188:191], v[56:59]\n\t"
  "v_mfma_f32_16x16x32_bf16 v[56:59], v[144:147], v[184:187], v[56:59]\n\t"
  "v_mfma_f32_16x16x32_bf16 v[60:63], v[152:155], v[184:187], v[60:63]\n\t"
  "v_mfma_f32_16x16x32_bf16 v[60:63], v[156:159], v[188:191], v[60:63]\n\t"
  "v_mfma_f32_16x16x32_bf16 v[44:47], v[156:159], v[180:183], v[44:47]\n\t"
  "v_mfma_f32_16x16x32_bf16 v[44:47], v[152:155], v[176:179], v[44:47]\n\t"
  "s_waitcnt lgkmcnt(3)\n\t"
  "v_mfma_f32_16x16x32_bf16 v[64:67], v[128:131], v[160:163], v[64:67]\n\t"
  "ds_read_b128 v[176:179], v192 offset:45056\n\t"
  "s_waitcnt lgkmcnt(3)\n\t"
  "v_mfma_f32_16x16x32_bf16 v[64:67], v[132:135], v[164:167], v[64:67]\n\t"
  "ds_read_b128 v[180:183], v193 offset:45056\n\t"
  "s_waitcnt lgkmcnt(2)\n\t"
  "v_mfma_f32_16x16x32_bf16 v[80:83], v[132:135], v[172:175], v[80:83]\n\t"
  "ds_read_b128 v[184:187], v192 offset:47104\n\t"
  "v_mfma_f32_16x16x32_bf16 v[80:83], v[128:131], v[168:171], v[80:83]\n\t"
  "ds_read_b128 v[188:191], v193 offset:47104\n\t"
  "v_mfma_f32_16x16x32_bf16 v[84:87], v[136:139], v[168:171], v[84:87]\n\t"
  "v_mfma_f32_16x16x32_bf16 v[84:87], v[140:143], v[172:175], v[84:87]\n\t"
  "v_mfma_f32_16x16x32_bf16 v[68:71], v[140:143], v[164:167], v[68:71]\n\t"
  "v_mfma_f32_16x16x32_bf16 v[68:71], v[136:139], v[160:163], v[68:71]\n\t"
  "v_mfma_f32_16x16x32_bf16 v[72:75], v[144:147], v[160:163], v[72:75]\n\t"
  "v_mfma_f32_16x16x32_bf16 v[72:75], v[148:151], v[164:167], v[72:75]\n\t"
  "v_mfma_f32_16x16x32_bf16 v[88:91], v[148:151], v[172:175], v[88:91]\n\t"
  "v_mfma_f32_16x16x32_bf16 v[88:91], v[144:147], v[168:171], v[88:91]\n\t"
  "v_mfma_f32_16x16x32_bf16 v[92:95], v[152:155], v[168:171], v[92:95]\n\t"
  "v_mfma_f32_16x16x32_bf16 v[92:95], v[156:159], v[172:175], v[92:95]\n\t"
  "v_mfma_f32_16x16x32_bf16 v[76:79], v[156:159], v[164:167], v[76:79]\n\t"
  "v_mfma_f32_16x16x32_bf16 v[76:79], v[152:155], v[160:163], v[76:79]\n\t"
  "s_waitcnt lgkmcnt(0)\n\t"
  "s_barrier\n\t"
  "v_mfma_f32_16x16x32_bf16 v[96:99], v[128:131], v[176:179], v[96:99]\n\t"
  "ds_read_b128 v[160:163], v192 offset:0\n\t"
  "v_mfma_f32_16x16x32_bf16 v[96:99], v[132:135], v[180:183], v[96:99]\n\t"
  "ds_read_b128 v[164:167], v193 offset:0\n\t"
  "v_mfma_f32_16x16x32_bf16 v[112:115], v[132:135], v[188:191], v[112:115]\n\t"
  "ds_read_b128 v[168:171], v192 offset:2048\n\t"
  "v_mfma_f32_16x16x32_bf16 v[112:115], v[128:131], v[184:187], v[112:115]\n\t"
  "ds_read_b128 v[172:175], v193 offset:2048\n\t"
  "v_mfma_f32_16x16x32_bf16 v[116:119], v[136:139], v[184:187], v[116:119]\n\t"
  "ds_read_b64_tr_b16 v[128:129], v194 offset:0\n\t"
  "v_mfma_f32_16x16x32_bf16 v[116:119], v[140:143], v[188:191], v[116:119]\n\t"
  "ds_read_b64_tr_b16 v[130:131], v194 offset:512\n\t"
  "v_mfma_f32_16x16x32_bf16 v[100:103], v[140:143], v[180:183], v[100:103]\n\t"
  "ds_read_b64_tr_b16 v[132:133], v194 offset:4096\n\t"
  "v_mfma_f32_16x16x32_bf16 v[100:103], v[136:139], v[176:179], v[100:103]\n\t"
  "ds_read_b64_tr_b16 v[134:135], v194 offset:4608\n\t"
  "v_mfma_f32_16x16x32_bf16 v[104:107], v[144:147], v[176:179], v[104:107]\n\t"
  "ds_read_b64_tr_b16 v[136:137], v195 offset:0\n\t"
  "v_mfma_f32_16x16x32_bf16 v[104:107], v[148:151], v[180:183], v[104:107]\n\t"
  "ds_read_b64_tr_b16 v[138:139], v195 offset:512\n\t"
  "v_mfma_f32_16x16x32_bf16 v[120:123], v[148:151], v[188:191], v[120:123]\n\t"
  "ds_read_b64_tr_b16 v[140:141], v195 offset:4096\n\t"
  "v_mfma_f32_16x16x32_bf16 v[120:123], v[144:147], v[184:187], v[120:123]\n\t"
  "ds_read_b64_tr_b16 v[142:143], v195 offset:4608\n\t"
  "v_mfma_f32_16x16x32_bf16 v[124:127], v[152:155], v[184:187], v[124:127]\n\t"
  "ds_read_b64_tr_b16 v[144:145], v196 offset:0\n\t"
  "v_mfma_f32_16x16x32_bf16 v[124:127], v[156:159], v[188:191], v[124:127]\n\t"
  "ds_read_b64_tr_b16 v[146:147], v196 offset:512\n\t"
  "v_mfma_f32_16x16x32_bf16 v[108:111], v[156:159], v[180:183], v[108:111]\n\t"
  "ds_read_b64_tr_b16 v[148:149], v196 offset:4096\n\t"
  "v_mfma_f32_16x16x32_bf16 v[108:111], v[152:155], v[176:179], v[108:111]\n\t"
  "ds_read_b64_tr_b16 v[150:151], v196 offset:4608\n\t"
  "s_waitcnt lgkmcnt(10)\n\t"
  "v_mfma_f32_16x16x32_bf16 v[0:3], v[128:131], v[160:163], v[0:3]\n\t"
  "ds_read_b64_tr_b16 v[152:153], v197 offset:0\n\t"
  "s_waitcnt lgkmcnt(9)\n\t"
  "v_mfma_f32_16x16x32_bf16 v[0:3], v[132:135], v[164:167], v[0:3]\n\t"
  "ds_read_b64_tr_b16 v[154:155], v197 offset:512\n\t"
  "v_mfma_f32_16x16x32_bf16 v[16:19], v[132:135], v[172:175], v[16:19]\n\t"
  "ds_read_b64_tr_b16 v[156:157], v197 offset:4096\n\t"
  "v_mfma_f32_16x16x32_bf16 v[16:19], v[128:131], v[168:171], v[16:19]\n\t"
  "ds_read_b64_tr_b16 v[158:159], v197 offset:4608\n\t"
  "s_waitcnt lgkmcnt(10)\n\t"
  "v_mfma_f32_16x16x32_bf16 v[20:23], v[136:139], v[168:171], v[20:23]\n\t"
  "ds_read_b128 v[176:179], v192 offset:4096\n\t"
  "s_waitcnt lgkmcnt(9)\n\t"
  "v_mfma_f32_16x16x32_bf16 v[20:23], v[140:143], v[172:175], v[20:23]\n\t"
  "ds_read_b128 v[180:183], v193 offset:4096\n\t"
  "v_mfma_f32_16x16x32_bf16 v[4:7], v[140:143], v[164:167], v[4:7]\n\t"
  "ds_read_b128 v[184:187], v192 offset:6144\n\t"
  "v_mfma_f32_16x16x32_bf16 v[4:7], v[136:139], v[160:163], v[4:7]\n\t"
  "ds_read_b128 v[188:191], v193 offset:6144\n\t"
  "s_waitcnt lgkmcnt(8)\n\t"
  "v_mfma_f32_16x16x32_bf16 v[8:11], v[144:147], v[160:163], v[8:11]\n\t"
  "v_mfma_f32_16x16x32_bf16 v[8:11], v[148:151], v[164:167], v[8:11]\n\t"
  "v_mfma_f32_16x16x32_bf16 v[24:27], v[148:151], v[172:175], v[24:27]\n\t"
  "v_mfma_f32_16x16x32_bf16 v[24:27], v[144:147], v[168:171], v[24:27]\n\t"
  "s_waitcnt lgkmcnt(4)\n\t"
  "v_mfma_f32_16x16x32_bf16 v[28:31], v[152:155], v[168:171], v[28:31]\n\t"
  "v_mfma_f32_16x16x32_bf16 v[28:31], v[156:159], v[172:175], v[28:31]\n\t"
  "v_mfma_f32_16x16x32_bf16 v[12:15], v[156:159], v[164:167], v[12:15]\n\t"
  "v_mfma_f32_16x16x32_bf16 v[12:15], v[152:155], v[160:163], v[12:15]\n\t"
  "s_waitcnt lgkmcnt(3)\n\t"
  "v_mfma_f32_16x16x32_bf16 v[32:35], v[128:131], v[176:179], v[32:35]\n\t"
  "ds_read_b128 v[160:163], v192 offset:8192\n\t"
  "s_waitcnt lgkmcnt(3)\n\t"
  "v_mfma_f32_16x16x32_bf16 v[32:35], v[132:135], v[180:183], v[32:35]\n\t"
  "ds_read_b128 v[164:167], v193 offset:8192\n\t"
  "s_waitcnt lgkmcnt(2)\n\t"
  "v_mfma_f32_16x16x32_bf16 v[48:51], v[132:135], v[188:191], v[48:51]\n\t"
  "ds_read_b128 v[168:171], v192 offset:10240\n\t"
  "v_mfma_f32_16x16x32_bf16 v[48:51], v[128:131], v[184:187], v[48:51]\n\t"
  "ds_read_b128 v[172:175], v193 offset:10240\n\t"
  "v_mfma_f32_16x16x32_bf16 v[52:55], v[136:139], v[184:187], v[52:55]\n\t"
  "v_mfma_f32_16x16x32_bf16 v[52:55], v[140:143], v[188:191], v[52:55]\n\t"
  "v_mfma_f32_16x16x32_bf16 v[36:39], v[140:143], v[180:183], v[36:39]\n\t"
  "v_mfma_f32_16x16x32_bf16 v[36:39], v[136:139], v[176:179], v[36:39]\n\t"
  "v_mfma_f32_16x16x32_bf16 v[40:43], v[144:147], v[176:179], v[40:43]\n\t"
  "v_mfma_f32_16x16x32_bf16 v[40:43], v[148:151], v[180:183], v[40:43]\n\t"
  "v_mfma_f32_16x16x32_bf16 v[56:59], v[148:151], v[188:191], v[56:59]\n\t"
  "v_mfma_f32_16x16x32_bf16 v[56:59], v[144:147], v[184:187], v[56:59]\n\t"
  "v_mfma_f32_16x16x32_bf16 v[60:63], v[152:155], v[184:187], v[60:63]\n\t"
  "v_mfma_f32_16x16x32_bf16 v[60:63], v[156:159], v[188:191], v[60:63]\n\t"
  "v_mfma_f32_16x16x32_bf16 v[44:47], v[156:159], v[180:183], v[44:47]\n\t"
  "v_mfma_f32_16x16x32_bf16 v[44:47], v[152:155], v[176:179], v[44:47]\n\t"
  "s_waitcnt lgkmcnt(3)\n\t"
  "v_mfma_f32_16x16x32_bf16 v[64:67], v[128:131], v[160:163], v[64:67]\n\t"
  "ds_read_b128 v[176:179], v192 offset:12288\n\t"
  "s_waitcnt lgkmcnt(3)\n\t"
  "v_mfma_f32_16x16x32_bf16 v[64:67], v[132:135], v[164:167], v[64:67]\n\t"
  "ds_read_b128 v[180:183], v193 offset:12288\n\t"
  "s_waitcnt lgkmcnt(2)\n\t"
  "v_mfma_f32_16x16x32_bf16 v[80:83], v[132:135], v[172:175], v[80:83]\n\t"
  "ds_read_b128 v[184:187], v192 offset:14336\n\t"
  "v_mfma_f32_16x16x32_bf16 v[80:83], v[128:131], v[168:171], v[80:83]\n\t"
  "ds_read_b128 v[188:191], v193 offset:14336\n\t"
  "v_mfma_f32_16x16x32_bf16 v[84:87], v[136:139], v[168:171], v[84:87]\n\t"
  "v_mfma_f32_16x16x32_bf16 v[84:87], v[140:143], v[172:175], v[84:87]\n\t"
  "v_mfma_f32_16x16x32_bf16 v[68:71], v[140:143], v[164:167], v[68:71]\n\t"
  "v_mfma_f32_16x16x32_bf16 v[68:71], v[136:139], v[160:163], v[68:71]\n\t"
  "v_mfma_f32_16x16x32_bf16 v[72:75], v[144:147], v[160:163], v[72:75]\n\t"
  "v_mfma_f32_16x16x32_bf16 v[72:75], v[148:151], v[164:167], v[72:75]\n\t"
  "v_mfma_f32_16x16x32_bf16 v[88:91], v[148:151], v[172:175], v[88:91]\n\t"
  "v_mfma_f32_16x16x32_bf16 v[88:91], v[144:147], v[168:171], v[88:91]\n\t"
  "v_mfma_f32_16x16x32_bf16 v[92:95], v[152:155], v[168:171], v[92:95]\n\t"
  "s_sub_u32 s40, s40, 1\n\t"
  "v_mfma_f32_16x16x32_bf16 v[92:95], v[156:159], v[172:175], v[92:95]\n\t"
  "s_cmp_eq_u32 s40, 0\n\t"
  "v_mfma_f32_16x16x32_bf16 v[76:79], v[156:159], v[164:167], v[76:79]\n\t"
  "v_mfma_f32_16x16x32_bf16 v[76:79], v[152:155], v[160:163], v[76:79]\n\t"
  "s_waitcnt lgkmcnt(0)\n\t"
  "s_cbranch_scc0 LPP%=\n\t"
  "s_barrier\n\t"
  "v_mfma_f32_16x16x32_bf16 v[96:99], v[128:131], v[176:179], v[96:99]\n\t"
  "ds_read_b128 v[160:163], v192 offset:16384\n\t"
  "v_mfma_f32_16x16x32_bf16 v[96:99], v[132:135], v[180:183], v[96:99]\n\t"
  "ds_read_b128 v[164:167], v193 offset:16384\n\t"
  "v_mfma_f32_16x16x32_bf16 v[112:115], v[132:135], v[188:191], v[112:115]\n\t"
  "ds_read_b128 v[168:171], v192 offset:18432\n\t"
  "v_mfma_f32_16x16x32_bf16 v[112:115], v[128:131], v[184:187], v[112:115]\n\t"
  "ds_read_b128 v[172:175], v193 offset:18432\n\t"
  "v_mfma_f32_16x16x32_bf16 v[116:119], v[136:139], v[184:187], v[116:119]\n\t"
  "ds_read_b64_tr_b16 v[128:129], v194 offset:8192\n\t"
  "v_mfma_f32_16x16x32_bf16 v[116:119], v[140:143], v[188:191], v[116:119]\n\t"
  "ds_read_b64_tr_b16 v[130:131], v194 offset:8704\n\t"
  "v_mfma_f32_16x16x32_bf16 v[100:103], v[140:143], v[180:183], v[100:103]\n\t"
  "ds_read_b64_tr_b16 v[132:133], v194 offset:12288\n\t"
  "v_mfma_f32_16x16x32_bf16 v[100:103], v[136:139], v[176:179], v[100:103]\n\t"
  "ds_read_b64_tr_b16 v[134:135], v194 offset:12800\n\t"
  "v_mfma_f32_16x16x32_bf16 v[104:107], v[144:147], v[176:179], v[104:107]\n\t"
  "ds_read_b64_tr_b16 v[136:137], v195 offset:8192\n\t"
  "v_mfma_f32_16x16x32_bf16 v[104:107], v[148:151], v[180:183], v[104:107]\n\t"
  "ds_read_b64_tr_b16 v[138:139], v195 offset:8704\n\t"
  "v_mfma_f32_16x16x32_bf16 v[120:123], v[148:151], v[188:191], v[120:123]\n\t"
  "ds_read_b64_tr_b16 v[140:141], v195 offset:12288\n\t"
  "v_mfma_f32_16x16x32_bf16 v[120:123], v[144:147], v[184:187], v[120:123]\n\t"
  "ds_read_b64_tr_b16 v[142:143], v195 offset:12800\n\t"
  "v_mfma_f32_16x16x32_bf16 v[124:127], v[152:155], v[184:187], v[124:127]\n\t"
  "ds_read_b64_tr_b16 v[144:145], v196 offset:8192\n\t"
  "v_mfma_f32_16x16x32_bf16 v[124:127], v[156:159], v[188:191], v[124:127]\n\t"
  "ds_read_b64_tr_b16 v[146:147], v196 offset:8704\n\t"
  "v_mfma_f32_16x16x32_bf16 v[108:111], v[156:159], v[180:183], v[108:111]\n\t"
  "ds_read_b64_tr_b16 v[148:149], v196 offset:12288\n\t"
  "v_mfma_f32_16x16x32_bf16 v[108:111], v[152:155], v[176:179], v[108:111]\n\t"
  "ds_read_b64_tr_b16 v[150:151], v196 offset:12800\n\t"
  "s_waitcnt lgkmcnt(10)\n\t"
  "v_mfma_f32_16x16x32_bf16 v[0:3], v[128:131], v[160:163], v[0:3]\n\t"
  "ds_read_b64_tr_b16 v[152:153], v197 offset:8192\n\t"
  "s_waitcnt lgkmcnt(9)\n\t"
  "v_mfma_f32_16x16x32_bf16 v[0:3], v[132:135], v[164:167], v[0:3]\n\t"
  "ds_read_b64_tr_b16 v[154:155], v197 offset:8704\n\t"
  "v_mfma_f32_16x16x32_bf16 v[16:19], v[132:135], v[172:175], v[16:19]\n\t"
  "ds_read_b64_tr_b16 v[156:157], v197 offset:12288\n\t"
  "v_mfma_f32_16x16x32_bf16 v[16:19], v[128:131], v[168:171], v[16:19]\n\t"
  "ds_read_b64_tr_b16 v[158:159], v197 offset:12800\n\t"
  "s_waitcnt lgkmcnt(10)\n\t"
  "v_mfma_f32_16x16x32_bf16 v[20:23], v[136:139], v[168:171], v[20:23]\n\t"
  "ds_read_b128 v[176:179], v192 offset:20480\n\t"
  "s_waitcnt lgkmcnt(9)\n\t"
  "v_mfma_f32_16x16x32_bf16 v[20:23], v[140:143], v[172:175], v[20:23]\n\t"
  "ds_read_b128 v[180:183], v193 offset:20480\n\t"
  "v_mfma_f32_16x16x32_bf16 v[4:7], v[140:143], v[164:167], v[4:7]\n\t"
  "ds_read_b128 v[184:187], v192 offset:22528\n\t"
  "v_mfma_f32_16x16x32_bf16 v[4:7], v[136:139], v[160:163], v[4:7]\n\t"
  "ds_read_b128 v[188:191], v193 offset:22528\n\t"
  "s_waitcnt lgkmcnt(10)\n\t"
  "v_mfma_f32_16x16x32_bf16 v[8:11], v[144:147], v[160:163], v[8:11]\n\t"
  "ds_read_b128 v[200:203], v198\n\t"
  "s_waitcnt lgkmcnt(9)\n\t"
  "v_mfma_f32_16x16x32_bf16 v[8:11], v[148:151], v[164:167], v[8:11]\n\t"
  "ds_read_b128 v[204:207], v198 offset:256\n\t"
  "v_mfma_f32_16x16x32_bf16 v[24:27], v[148:151], v[172:175], v[24:27]\n\t"
  "v_mfma_f32_16x16x32_bf16 v[24:27], v[144:147], v[168:171], v[24:27]\n\t"
  "s_waitcnt lgkmcnt(6)\n\t"
  "v_mfma_f32_16x16x32_bf16 v[28:31], v[152:155], v[168:171], v[28:31]\n\t"
  "v_mfma_f32_16x16x32_bf16 v[28:31], v[156:159], v[172:175], v[28:31]\n\t"
  "v_mfma_f32_16x16x32_bf16 v[12:15], v[156:159], v[164:167], v[12:15]\n\t"
  "v_mfma_f32_16x16x32_bf16 v[12:15], v[152:155], v[160:163], v[12:15]\n\t"
  "s_waitcnt lgkmcnt(0)\n\t"
  "v_mfma_f32_16x16x32_bf16 v[32:35], v[128:131], v[176:179], v[32:35]\n\t"
  "ds_read_b128 v[160:163], v192 offset:24576\n\t"
  "v_mfma_f32_16x16x32_bf16 v[32:35], v[132:135], v[180:183], v[32:35]\n\t"
  "v_mfma_f32_16x16x32_bf16 v[48:51], v[132:135], v[188:191], v[48:51]\n\t"
  "v_mfma_f32_16x16x32_bf16 v[48:51], v[128:131], v[184:187], v[48:51]\n\t"
  "v_mfma_f32_16x16x32_bf16 v[52:55], v[136:139], v[184:187], v[52:55]\n\t"
  "ds_read_b128 v[164:167], v193 offset:24576\n\t"
  "v_mfma_f32_16x16x32_bf16 v[52:55], v[140:143], v[188:191], v[52:55]\n\t"
  "v_mfma_f32_16x16x32_bf16 v[36:39], v[140:143], v[180:183], v[36:39]\n\t"
  "v_mfma_f32_16x16x32_bf16 v[36:39], v[136:139], v[176:179], v[36:39]\n\t"
  "v_mfma_f32_16x16x32_bf16 v[40:43], v[144:147], v[176:179], v[40:43]\n\t"
  "ds_read_b128 v[168:171], v192 offset:26624\n\t"
  "v_mfma_f32_16x16x32_bf16 v[40:43], v[148:151], v[180:183], v[40:43]\n\t"
  "v_mfma_f32_16x16x32_bf16 v[56:59], v[148:151], v[188:191], v[56:59]\n\t"
  "v_mfma_f32_16x16x32_bf16 v[56:59], v[144:147], v[184:187], v[56:59]\n\t"
  "v_mfma_f32_16x16x32_bf16 v[60:63], v[152:155], v[184:187], v[60:63]\n\t"
  "ds_read_b128 v[172:175], v193 offset:26624\n\t"
  "v_mfma_f32_16x16x32_bf16 v[60:63], v[156:159], v[188:191], v[60:63]\n\t"
  "v_mfma_f32_16x16x32_bf16 v[44:47], v[156:159], v[180:183], v[44:47]\n\t"
  "v_mfma_f32_16x16x32_bf16 v[44:47], v[152:155], v[176:179], v[44:47]\n\t"
  "s_waitcnt lgkmcnt(0)\n\t"
  "v_mfma_f32_16x16x32_bf16 v[64:67], v[128:131], v[160:163], v[64:67]\n\t"
  "ds_read_b128 v[176:179], v192 offset:28672\n\t"
  "ds_read_b128 v[180:183], v193 offset:28672\n\t"
  "ds_read_b128 v[184:187], v192 offset:30720\n\t"
  "v_mfma_f32_16x16x32_bf16 v[64:67], v[132:135], v[164:167], v[64:67]\n\t"
  "ds_read_b128 v[188:191], v193 offset:30720\n\t"
  "s_add_u32 s41, %[so0], 0\n\t"
  "v_mfma_f32_16x16x32_bf16 v[80:83], v[132:135], v[172:175], v[80:83]\n\t"
  "v_mul_f32 v0, v0, v200\n\t"
  "v_mul_f32 v1, v1, v200\n\t"
  "v_mfma_f32_16x16x32_bf16 v[80:83], v[128:131], v[168:171], v[80:83]\n\t"
  "v_mul_f32 v2, v2, v200\n\t"
  "v_mul_f32 v3, v3, v200\n\t"
  "v_mfma_f32_16x16x32_bf16 v[84:87], v[136:139], v[168:171], v[84:87]\n\t"
  "v_mul_f32 v4, v4, v200\n\t"
  "v_mul_f32 v5, v5, v200\n\t"
  "v_mfma_f32_16x16x32_bf16 v[84:87], v[140:143], v[172:175], v[84:87]\n\t"
  "v_mul_f32 v6, v6, v200\n\t"
  "v_mul_f32 v7, v7, v200\n\t"
  "v_cvt_pk_bf16_f32 v208, v0, v1\n\t"
  "v_mfma_f32_16x16x32_bf16 v[68:71], v[140:143], v[164:167], v[68:71]\n\t"
  "v_cvt_pk_bf16_f32 v210, v4, v5\n\t"
  "v_cvt_pk_bf16_f32 v209, v2, v3\n\t"
  "v_mfma_f32_16x16x32_bf16 v[68:71], v[136:139], v[160:163], v[68:71]\n\t"
  "v_cvt_pk_bf16_f32 v211, v6, v7\n\t"
  "v_permlane16_swap_b32 v208, v210\n\t"
  "v_mfma_f32_16x16x32_bf16 v[72:75], v[144:147], v[160:163], v[72:75]\n\t"
  "v_permlane16_swap_b32 v209, v211\n\t"
  "buffer_store_dwordx4 v[208:211], v199, %[srd], s41 offen offset:0 sc1\n\t"
  "v_mfma_f32_16x16x32_bf16 v[72:75], v[148:151], v[164:167], v[72:75]\n\t"
  "v_mul_f32 v8, v8, v200\n\t"
  "v_mul_f32 v9, v9, v200\n\t"
  "v_mfma_f32_16x16x32_bf16 v[88:91], v[148:151], v[172:175], v[88:91]\n\t"
  "v_mul_f32 v10, v10, v200\n\t"
  "v_mul_f32 v11, v11, v200\n\t"
  "v_mul_f32 v12, v12, v200\n\t"
  "v_mfma_f32_16x16x32_bf16 v[88:91], v[144:147], v[168:171], v[88:91]\n\t"
  "v_mul_f32 v13, v13, v200\n\t"
  "v_mul_f32 v14, v14, v200\n\t"
  "v_mfma_f32_16x16x32_bf16 v[92:95], v[152:155], v[168:171], v[92:95]\n\t"
  "v_mul_f32 v15, v15, v200\n\t"
  "v_cvt_pk_bf16_f32 v212, v8, v9\n\t"
  "v_mfma_f32_16x16x32_bf16 v[92:95], v[156:159], v[172:175], v[92:95]\n\t"
  "v_cvt_pk_bf16_f32 v214, v12, v13\n\t"
  "v_cvt_pk_bf16_f32 v213, v10, v11\n\t"
  "v_mfma_f32_16x16x32_bf16 v[76:79], v[156:159], v[164:167], v[76:79]\n\t"
  "v_cvt_pk_bf16_f32 v215, v14, v15\n\t"
  "v_permlane16_swap_b32 v212, v214\n\t"
  "v_mfma_f32_16x16x32_bf16 v[76:79], v[152:155], v[160:163], v[76:79]\n\t"
  "v_permlane16_swap_b32 v213, v215\n\t"
  "buffer_store_dwordx4 v[212:215], v199, %[srd], s41 offen offset:1024 sc1\n\t"
  "s_waitcnt lgkmcnt(0)\n\t"
  "s_barrier\n\t"
  "v_mfma_f32_16x16x32_bf16 v[96:99], v[128:131], v[176:179], v[96:99]\n\t"
  "v_mul_f32 v16, v16, v201\n\t"
  "v_mul_f32 v17, v17, v201\n\t"
  "v_mfma_f32_16x16x32_bf16 v[96:99], v[132:135], v[180:183], v[96:99]\n\t"
  "v_mul_f32 v18, v18, v201\n\t"
  "v_mul_f32 v19, v19, v201\n\t"
  "v_mfma_f32_16x16x32_bf16 v[112:115], v[132:135], v[188:191], v[112:115]\n\t"
  "v_mul_f32 v20, v20, v201\n\t"
  "v_mul_f32 v21, v21, v201\n\t"
  "v_mfma_f32_16x16x32_bf16 v[112:115], v[128:131], v[184:187], v[112:115]\n\t"
  "v_mul_f32 v22, v22, v201\n\t"
  "v_mul_f32 v23, v23, v201\n\t"
  "v_mfma_f32_16x16x32_bf16 v[116:119], v[136:139], v[184:187], v[116:119]\n\t"
  "v_cvt_pk_bf16_f32 v216, v16, v17\n\t"
  "v_cvt_pk_bf16_f32 v218, v20, v21\n\t"
  "v_mfma_f32_16x16x32_bf16 v[116:119], v[140:143], v[188:191], v[116:119]\n\t"
  "v_cvt_pk_bf16_f32 v217, v18, v19\n\t"
  "v_cvt_pk_bf16_f32 v219, v22, v23\n\t"
  "v_mfma_f32_16x16x32_bf16 v[100:103], v[140:143], v[180:183], v[100:103]\n\t"
  "v_permlane16_swap_b32 v216, v218\n\t"
  "v_permlane16_swap_b32 v217, v219\n\t"
  "v_mfma_f32_16x16x32_bf16 v[100:103], v[136:139], v[176:179], v[100:103]\n\t"
  "buffer_store_dwordx4 v[216:219], v199, %[srd], s41 offen offset:2048 sc1\n\t"
  "v_mfma_f32_16x16x32_bf16 v[104:107], v[144:147], v[176:179], v[104:107]\n\t"
  "v_mul_f32 v24, v24, v201\n\t"
  "v_mul_f32 v25, v25, v201\n\t"
  "v_mfma_f32_16x16x32_bf16 v[104:107], v[148:151], v[180:183], v[104:107]\n\t"
  "v_mul_f32 v26, v26, v201\n\t"
  "v_mul_f32 v27, v27, v201\n\t"
  "v_mfma_f32_16x16x32_bf16 v[120:123], v[148:151], v[188:191], v[120:123]\n\t"
  "v_mul_f32 v28, v28, v201\n\t"
  "v_mul_f32 v29, v29, v201\n\t"
  "v_mfma_f32_16x16x32_bf16 v[120:123], v[144:147], v[184:187], v[120:123]\n\t"
  "v_mul_f32 v30, v30, v201\n\t"
  "v_mul_f32 v31, v31, v201\n\t"
  "v_mfma_f32_16x16x32_bf16 v[124:127], v[152:155], v[184:187], v[124:127]\n\t"
  "v_cvt_pk_bf16_f32 v220, v24, v25\n\t"
  "v_cvt_pk_bf16_f32 v222, v28, v29\n\t"
  "v_mfma_f32_16x16x32_bf16 v[124:127], v[156:159], v[188:191], v[124:127]\n\t"
  "v_cvt_pk_bf16_f32 v221, v26, v27\n\t"
  "v_cvt_pk_bf16_f32 v223, v30, v31\n\t"
  "v_mfma_f32_16x16x32_bf16 v[108:111], v[156:159], v[180:183], v[108:111]\n\t"
  "v_permlane16_swap_b32 v220, v222\n\t"
  "v_permlane16_swap_b32 v221, v223\n\t"
  "v_mfma_f32_16x16x32_bf16 v[108:111], v[152:155], v[176:179], v[108:111]\n\t"
  "buffer_store_dwordx4 v[220:223], v199, %[srd], s41 offen offset:3072 sc1\n\t"
  "s_add_u32 s41, %[so0], 4096\n\t"
  "v_mul_f32 v32, v32, v202\n\t"
  "v_mul_f32 v33, v33, v202\n\t"
  "v_mul_f32 v34, v34, v202\n\t"
  "v_mul_f32 v35, v35, v202\n\t"
  "v_mul_f32 v36, v36, v202\n\t"
  "v_mul_f32 v37, v37, v202\n\t"
  "v_mul_f32 v38, v38, v202\n\t"
  "v_mul_f32 v39, v39, v202\n\t"
  "v_cvt_pk_bf16_f32 v224, v32, v33\n\t"
  "v_cvt_pk_bf16_f32 v226, v36, v37\n\t"
  "v_cvt_pk_bf16_f32 v225, v34, v35\n\t"
  "v_cvt_pk_bf16_f32 v227, v38, v39\n\t"
  "v_permlane16_swap_b32 v224, v226\n\t"
  "s_nop 0\n\t"
  "v_permlane16_swap_b32 v225, v227\n\t"
  "buffer_store_dwordx4 v[224:227], v199, %[srd], s41 offen offset:0 sc1\n\t"
  "v_mul_f32 v40, v40, v202\n\t"
  "v_mul_f32 v41, v41, v202\n\t"
  "v_mul_f32 v42, v42, v202\n\t"
  "v_mul_f32 v43, v43, v202\n\t"
  "v_mul_f32 v44, v44, v202\n\t"
  "v_mul_f32 v45, v45, v202\n\t"
  "v_mul_f32 v46, v46, v202\n\t"
  "v_mul_f32 v47, v47, v202\n\t"
  "v_cvt_pk_bf16_f32 v228, v40, v41\n\t"
  "v_cvt_pk_bf16_f32 v230, v44, v45\n\t"
  "v_cvt_pk_bf16_f32 v229, v42, v43\n\t"
  "v_cvt_pk_bf16_f32 v231, v46, v47\n\t"
  "v_permlane16_swap_b32 v228, v230\n\t"
  "s_nop 0\n\t"
  "v_permlane16_swap_b32 v229, v231\n\t"
  "buffer_store_dwordx4 v[228:231], v199, %[srd], s41 offen offset:1024 sc1\n\t"
  "v_mul_f32 v48, v48, v203\n\t"
  "v_mul_f32 v49, v49, v203\n\t"
  "v_mul_f32 v50, v50, v203\n\t"
  "v_mul_f32 v51, v51, v203\n\t"
  "v_mul_f32 v52, v52, v203\n\t"
  "v_mul_f32 v53, v53, v203\n\t"
  "v_mul_f32 v54, v54, v203\n\t"
  "v_mul_f32 v55, v55, v203\n\t"
  "v_cvt_pk_bf16_f32 v232, v48, v49\n\t"
  "v_cvt_pk_bf16_f32 v234, v52, v53\n\t"
  "v_cvt_pk_bf16_f32 v233, v50, v51\n\t"
  "v_cvt_pk_bf16_f32 v235, v54, v55\n\t"
  "v_permlane16_swap_b32 v232, v234\n\t"
  "s_nop 0\n\t"
  "v_permlane16_swap_b32 v233, v235\n\t"
  "buffer_store_dwordx4 v[232:235], v199, %[srd], s41 offen offset:2048 sc1\n\t"
  "v_mul_f32 v56, v56, v203\n\t"
  "v_mul_f32 v57, v57, v203\n\t"
  "v_mul_f32 v58, v58, v203\n\t"
  "v_mul_f32 v59, v59, v203\n\t"
  "v_mul_f32 v60, v60, v203\n\t"
  "v_mul_f32 v61, v61, v203\n\t"
  "v_mul_f32 v62, v62, v203\n\t"
  "v_mul_f32 v63, v63, v203\n\t"
  "v_cvt_pk_bf16_f32 v236, v56, v57\n\t"
  "v_cvt_pk_bf16_f32 v238, v60, v61\n\t"
  "v_cvt_pk_bf16_f32 v237, v58, v59\n\t"
  "v_cvt_pk_bf16_f32 v239, v62, v63\n\t"
  "v_permlane16_swap_b32 v236, v238\n\t"
  "s_nop 0\n\t"
  "v_permlane16_swap_b32 v237, v239\n\t"
  "buffer_store_dwordx4 v[236:239], v199, %[srd], s41 offen offset:3072 sc1\n\t"
  "s_add_u32 s41, %[so0], 8192\n\t"
  "v_mul_f32 v64, v64, v204\n\t"
  "v_mul_f32 v65, v65, v204\n\t"
  "v_mul_f32 v66, v66, v204\n\t"
  "v_mul_f32 v67, v67, v204\n\t"
  "v_mul_f32 v68, v68, v204\n\t"
  "v_mul_f32 v69, v69, v204\n\t"
  "v_mul_f32 v70, v70, v204\n\t"
  "v_mul_f32 v71, v71, v204\n\t"
  "v_cvt_pk_bf16_f32 v240, v64, v65\n\t"
  "v_cvt_pk_bf16_f32 v242, v68, v69\n\t"
  "v_cvt_pk_bf16_f32 v241, v66, v67\n\t"
  "v_cvt_pk_bf16_f32 v243, v70, v71\n\t"
  "v_permlane16_swap_b32 v240, v242\n\t"
  "s_nop 0\n\t"
  "v_permlane16_swap_b32 v241, v243\n\t"
  "buffer_store_dwordx4 v[240:243], v199, %[srd], s41 offen offset:0 sc1\n\t"
  "v_mul_f32 v72, v72, v204\n\t"
  "v_mul_f32 v73, v73, v204\n\t"
  "v_mul_f32 v74, v74, v204\n\t"
  "v_mul_f32 v75, v75, v204\n\t"
  "v_mul_f32 v76, v76, v204\n\t"
  "v_mul_f32 v77, v77, v204\n\t"
  "v_mul_f32 v78, v78, v204\n\t"
  "v_mul_f32 v79, v79, v204\n\t"
  "v_cvt_pk_bf16_f32 v244, v72, v73\n\t"
  "v_cvt_pk_bf16_f32 v246, v76, v77\n\t"
  "v_cvt_pk_bf16_f32 v245, v74, v75\n\t"
  "v_cvt_pk_bf16_f32 v247, v78, v79\n\t"
  "v_permlane16_swap_b32 v244, v246\n\t"
  "s_nop 0\n\t"
  "v_permlane16_swap_b32 v245, v247\n\t"
  "buffer_store_dwordx4 v[244:247], v199, %[srd], s41 offen offset:1024 sc1\n\t"
  "v_mul_f32 v80, v80, v205\n\t"
  "v_mul_f32 v81, v81, v205\n\t"
  "v_mul_f32 v82, v82, v205\n\t"
  "v_mul_f32 v83, v83, v205\n\t"
  "v_mul_f32 v84, v84, v205\n\t"
  "v_mul_f32 v85, v85, v205\n\t"
  "v_mul_f32 v86, v86, v205\n\t"
  "v_mul_f32 v87, v87, v205\n\t"
  "v_cvt_pk_bf16_f32 v248, v80, v81\n\t"
  "v_cvt_pk_bf16_f32 v250, v84, v85\n\t"
  "v_cvt_pk_bf16_f32 v249, v82, v83\n\t"
  "v_cvt_pk_bf16_f32 v251, v86, v87\n\t"
  "v_permlane16_swap_b32 v248, v250\n\t"
  "s_nop 0\n\t"
  "v_permlane16_swap_b32 v249, v251\n\t"
  "buffer_store_dwordx4 v[248:251], v199, %[srd], s41 offen offset:2048 sc1\n\t"
  "v_mul_f32 v88, v88, v205\n\t"
  "v_mul_f32 v89, v89, v205\n\t"
  "v_mul_f32 v90, v90, v205\n\t"
  "v_mul_f32 v91, v91, v205\n\t"
  "v_mul_f32 v92, v92, v205\n\t"
  "v_mul_f32 v93, v93, v205\n\t"
  "v_mul_f32 v94, v94, v205\n\t"
  "v_mul_f32 v95, v95, v205\n\t"
  "v_cvt_pk_bf16_f32 v252, v88, v89\n\t"
  "v_cvt_pk_bf16_f32 v254, v92, v93\n\t"
  "v_cvt_pk_bf16_f32 v253, v90, v91\n\t"
  "v_cvt_pk_bf16_f32 v255, v94, v95\n\t"
  "v_permlane16_swap_b32 v252, v254\n\t"
  "s_nop 0\n\t"
  "v_permlane16_swap_b32 v253, v255\n\t"
  "buffer_store_dwordx4 v[252:255], v199, %[srd], s41 offen offset:3072 sc1\n\t"
  "s_add_u32 s41, %[so0], 12288\n\t"
  "v_mul_f32 v96, v96, v206\n\t"
  "v_mul_f32 v97, v97, v206\n\t"
  "v_mul_f32 v98, v98, v206\n\t"
  "v_mul_f32 v99, v99, v206\n\t"
  "v_mul_f32 v100, v100, v206\n\t"
  "v_mul_f32 v101, v101, v206\n\t"
  "v_mul_f32 v102, v102, v206\n\t"
  "v_mul_f32 v103, v103, v206\n\t"
  "v_cvt_pk_bf16_f32 v208, v96, v97\n\t"
  "v_cvt_pk_bf16_f32 v210, v100, v101\n\t"
  "v_cvt_pk_bf16_f32 v209, v98, v99\n\t"
  "v_cvt_pk_bf16_f32 v211, v102, v103\n\t"
  "v_permlane16_swap_b32 v208, v210\n\t"
  "s_nop 0\n\t"
  "v_permlane16_swap_b32 v209, v211\n\t"
  "buffer_store_dwordx4 v[208:211], v199, %[srd], s41 offen offset:0 sc1\n\t"
  "v_mul_f32 v104, v104, v206\n\t"
  "v_mul_f32 v105, v105, v206\n\t"
  "v_mul_f32 v106, v106, v206\n\t"
  "v_mul_f32 v107, v107, v206\n\t"
  "v_mul_f32 v108, v108, v206\n\t"
  "v_mul_f32 v109, v109, v206\n\t"
  "v_mul_f32 v110, v110, v206\n\t"
  "v_mul_f32 v111, v111, v206\n\t"
  "v_cvt_pk_bf16_f32 v212, v104, v105\n\t"
  "v_cvt_pk_bf16_f32 v214, v108, v109\n\t"
  "v_cvt_pk_bf16_f32 v213, v106, v107\n\t"
  "v_cvt_pk_bf16_f32 v215, v110, v111\n\t"
  "v_permlane16_swap_b32 v212, v214\n\t"
  "s_nop 0\n\t"
  "v_permlane16_swap_b32 v213, v215\n\t"
  "buffer_store_dwordx4 v[212:215], v199, %[srd], s41 offen offset:1024 sc1\n\t"
  "v_mul_f32 v112, v112, v207\n\t"
  "v_mul_f32 v113, v113, v207\n\t"
  "v_mul_f32 v114, v114, v207\n\t"
  "v_mul_f32 v115, v115, v207\n\t"
  "v_mul_f32 v116, v116, v207\n\t"
  "v_mul_f32 v117, v117, v207\n\t"
  "v_mul_f32 v118, v118, v207\n\t"
  "v_mul_f32 v119, v119, v207\n\t"
  "v_cvt_pk_bf16_f32 v216, v112, v113\n\t"
  "v_cvt_pk_bf16_f32 v218, v116, v117\n\t"
  "v_cvt_pk_bf16_f32 v217, v114, v115\n\t"
  "v_cvt_pk_bf16_f32 v219, v118, v119\n\t"
  "v_permlane16_swap_b32 v216, v218\n\t"
  "s_nop 0\n\t"
  "v_permlane16_swap_b32 v217, v219\n\t"
  "buffer_store_dwordx4 v[216:219], v199, %[srd], s41 offen offset:2048 sc1\n\t"
  "v_mul_f32 v120, v120, v207\n\t"
  "v_mul_f32 v121, v121, v207\n\t"
  "v_mul_f32 v122, v122, v207\n\t"
  "v_mul_f32 v123, v123, v207\n\t"
  "v_mul_f32 v124, v124, v207\n\t"
  "v_mul_f32 v125, v125, v207\n\t"
  "v_mul_f32 v126, v126, v207\n\t"
  "v_mul_f32 v127, v127, v207\n\t"
  "v_cvt_pk_bf16_f32 v220, v120, v121\n\t"
  "v_cvt_pk_bf16_f32 v222, v124, v125\n\t"
  "v_cvt_pk_bf16_f32 v221, v122, v123\n\t"
  "v_cvt_pk_bf16_f32 v223, v126, v127\n\t"
  "v_permlane16_swap_b32 v220, v222\n\t"
  "s_nop 0\n\t"
  "v_permlane16_swap_b32 v221, v223\n\t"
  "buffer_store_dwordx4 v[220:223], v199, %[srd], s41 offen offset:3072 sc1\n\t"
  "s_nop 1\n\t"
  :: "{v192}"(a0), "{v193}"(a0 ^ 64), "{v194}"(b0), "{v195}"(b0 ^ 32), "{v196}"(b0 ^ 64), "{v197}"(b0 ^ 96), "{v198}"(ga), "{v199}"(vo), [srd]"s"(sO), [so0]"s"(so0) : "memory","scc","s40","s41","v0","v1","v2","v3","v4","v5","v6","v7","v8","v9","v10","v11","v12","v13","v14","v15","v16","v17","v18","v19","v20","v21","v22","v23","v24","v25","v26","v27","v28","v29","v30","v31","v32","v33","v34","v35","v36","v37","v38","v39","v40","v41","v42","v43","v44","v45","v46","v47","v48","v49","v50","v51","v52","v53","v54","v55","v56","v57","v58","v59","v60","v61","v62","v63","v64","v65","v66","v67","v68","v69","v70","v71","v72","v73","v74","v75","v76","v77","v78","v79","v80","v81","v82","v83","v84","v85","v86","v87","v88","v89","v90","v91","v92","v93","v94","v95","v96","v97","v98","v99","v100","v101","v102","v103","v104","v105","v106","v107","v108","v109","v110","v111","v112","v113","v114","v115","v116","v117","v118","v119","v120","v121","v122","v123","v124","v125","v126","v127","v128","v129","v130","v131","v132","v133","v134","v135","v136","v137","v138","v139","v140","v141","v142","v143","v144","v145","v146","v147","v148","v149","v150","v151","v152","v153","v154","v155","v156","v157","v158","v159","v160","v161","v162","v163","v164","v165","v166","v167","v168","v169","v170","v171","v172","v173","v174","v175","v176","v177","v178","v179","v180","v181","v182","v183","v184","v185","v186","v187","v188","v189","v190","v191","v200","v201","v202","v203","v204","v205","v206","v207","v208","v209","v210","v211","v212","v213","v214","v215","v216","v217","v218","v219","v220","v221","v222","v223","v224","v225","v226","v227","v228","v229","v230","v231","v232","v233","v234","v235","v236","v237","v238","v239","v240","v241","v242","v243","v244","v245","v246","v247","v248","v249","v250","v251","v252","v253","v254","v255");
    } else {
      f32x32 ac[6];
      const unsigned xb = L_X + wid * 2048;
      const i32x4 sO = mksrd(a2, 12u << 20);
      const unsigned so0 = (2u << 22) + (pan * 2 + wm) * 262144 + hp * 16384 + wn * 8192 - xb;
      u64 mc0, mc1, mnp;
      {
        LANES;
        mc0 = __ballot(4 * g + (l15 & 1) <= l15); mc1 = __ballot(4 * g + 2 + (l15 & 1) <= l15); mnp = __ballot(pan == 0 && wid == 0);
        const unsigned a0 = wid * 24576 + l15 * 128 + ((g ^ (lane & 7)) << 4);
        const unsigned b0 = L_QB + (8 * g + q) * 128 + (f << 5) + p * 8;
        asm volatile(
  "v_mov_b64 v[0:1], 0\n\t"
  "v_mov_b64 v[2:3], 0\n\t"
  "v_mov_b64 v[4:5], 0\n\t"
  "v_mov_b64 v[6:7], 0\n\t"
  "v_mov_b64 v[8:9], 0\n\t"
  "v_mov_b64 v[10:11], 0\n\t"
  "v_mov_b64 v[12:13], 0\n\t"
  "v_mov_b64 v[14:15], 0\n\t"
  "v_mov_b64 v[16:17], 0\n\t"
  "v_mov_b64 v[18:19], 0\n\t"
  "v_mov_b64 v[20:21], 0\n\t"
  "v_mov_b64 v[22:23], 0\n\t"
  "v_mov_b64 v[24:25], 0\n\t"
  "v_mov_b64 v[26:27], 0\n\t"
  "v_mov_b64 v[28:29], 0\n\t"
  "v_mov_b64 v[30:31], 0\n\t"
  "v_mov_b64 v[32:33], 0\n\t"
  "v_mov_b64 v[34:35], 0\n\t"
  "v_mov_b64 v[36:37], 0\n\t"
  "v_mov_b64 v[38:39], 0\n\t"
  "v_mov_b64 v[40:41], 0\n\t"
  "v_mov_b64 v[42:43], 0\n\t"
  "v_mov_b64 v[44:45], 0\n\t"
  "v_mov_b64 v[46:47], 0\n\t"
  "v_mov_b64 v[48:49], 0\n\t"
  "v_mov_b64 v[50:51], 0\n\t"
  "v_mov_b64 v[52:53], 0\n\t"
  "v_mov_b64 v[54:55], 0\n\t"
  "v_mov_b64 v[56:57], 0\n\t"
  "v_mov_b64 v[58:59], 0\n\t"
  "v_mov_b64 v[60:61], 0\n\t"
  "v_mov_b64 v[62:63], 0\n\t"
  "v_mov_b64 v[64:65], 0\n\t"
  "v_mov_b64 v[66:67], 0\n\t"
  "v_mov_b64 v[68:69], 0\n\t"
  "v_mov_b64 v[70:71], 0\n\t"
  "v_mov_b64 v[72:73], 0\n\t"
  "v_mov_b64 v[74:75], 0\n\t"
  "v_mov_b64 v[76:77], 0\n\t"
  "v_mov_b64 v[78:79], 0\n\t"
  "v_mov_b64 v[80:81], 0\n\t"
  "v_mov_b64 v[82:83], 0\n\t"
  "v_mov_b64 v[84:85], 0\n\t"
  "v_mov_b64 v[86:87], 0\n\t"
  "v_mov_b64 v[88:89], 0\n\t"
  "v_mov_b64 v[90:91], 0\n\t"
  "v_mov_b64 v[92:93], 0\n\t"
  "v_mov_b64 v[94:95], 0\n\t"
  "v_mov_b64 v[96:97], 0\n\t"
  "v_mov_b64 v[98:99], 0\n\t"
  "v_mov_b64 v[100:101], 0\n\t"
  "v_mov_b64 v[102:103], 0\n\t"
  "v_mov_b64 v[104:105], 0\n\t"
  "v_mov_b64 v[106:107], 0\n\t"
  "v_mov_b64 v[108:109], 0\n\t"
  "v_mov_b64 v[110:111], 0\n\t"
  "v_mov_b64 v[112:113], 0\n\t"
  "v_mov_b64 v[114:115], 0\n\t"
  "v_mov_b64 v[116:117], 0\n\t"
  "v_mov_b64 v[118:119], 0\n\t"
  "v_mov_b64 v[120:121], 0\n\t"
  "v_mov_b64 v[122:123], 0\n\t"
  "v_mov_b64 v[124:125], 0\n\t"
  "v_mov_b64 v[126:127], 0\n\t"
  "v_mov_b64 v[128:129], 0\n\t"
  "v_mov_b64 v[130:131], 0\n\t"
  "v_mov_b64 v[132:133], 0\n\t"
  "v_mov_b64 v[134:135], 0\n\t"
  "v_mov_b64 v[136:137], 0\n\t"
  "v_mov_b64 v[138:139], 0\n\t"
  "v_mov_b64 v[140:141], 0\n\t"
  "v_mov_b64 v[142:143], 0\n\t"
  "v_mov_b64 v[144:145], 0\n\t"
  "v_mov_b64 v[146:147], 0\n\t"
  "v_mov_b64 v[148:149], 0\n\t"
  "v_mov_b64 v[150:151], 0\n\t"
  "v_mov_b64 v[152:153], 0\n\t"
  "v_mov_b64 v[154:155], 0\n\t"
  "v_mov_b64 v[156:157], 0\n\t"
  "v_mov_b64 v[158:159], 0\n\t"
  "v_mov_b64 v[160:161], 0\n\t"
  "v_mov_b64 v[162:163], 0\n\t"
  "v_mov_b64 v[164:165], 0\n\t"
  "v_mov_b64 v[166:167], 0\n\t"
  "v_mov_b64 v[168:169], 0\n\t"
  "v_mov_b64 v[170:171], 0\n\t"
  "v_mov_b64 v[172:173], 0\n\t"
  "v_mov_b64 v[174:175], 0\n\t"
  "v_mov_b64 v[176:177], 0\n\t"
  "v_mov_b64 v[178:179], 0\n\t"
  "v_mov_b64 v[180:181], 0\n\t"
  "v_mov_b64 v[182:183], 0\n\t"
  "v_mov_b64 v[184:185], 0\n\t"
  "v_mov_b64 v[186:187], 0\n\t"
  "v_mov_b64 v[188:189], 0\n\t"
  "v_mov_b64 v[190:191], 0\n\t"
  "s_barrier\n\t"
  "ds_read_b128 v[192:195], v248 offset:0\n\t"
  "ds_read_b128 v[196:199], v249 offset:0\n\t"
  "ds_read_b64_tr_b16 v[224:225], v250 offset:0\n\t"
  "ds_read_b64_tr_b16 v[226:227], v250 offset:512\n\t"
  "ds_read_b64_tr_b16 v[228:229], v250 offset:4096\n\t"
  "ds_read_b64_tr_b16 v[230:231], v250 offset:4608\n\t"
  "ds_read_b128 v[200:203], v248 offset:2048\n\t"
  "ds_read_b128 v[204:207], v249 offset:2048\n\t"
  "ds_read_b128 v[208:211], v248 offset:4096\n\t"
  "ds_read_b128 v[212:215], v249 offset:4096\n\t"
  "ds_read_b128 v[216:219], v248 offset:6144\n\t"
  "ds_read_b128 v[220:223], v249 offset:6144\n\t"
  "ds_read_b64_tr_b16 v[232:233], v251 offset:0\n\t"
  "ds_read_b64_tr_b16 v[234:235], v251 offset:512\n\t"
  "ds_read_b64_tr_b16 v[236:237], v251 offset:4096\n\t"
  "ds_read_b64_tr_b16 v[238:239], v251 offset:4608\n\t"
  "s_mov_b32 s40, 3\n\t"
  ".p2align 6\n\t"
  "LP%=:\n\t"
  "s_waitcnt lgkmcnt(10)\n\t"
  "v_mfma_f32_16x16x32_bf16 v[0:3], v[224:227], v[192:195], v[0:3]\n\t"
  "ds_read_b64_tr_b16 v[240:241], v250 offset:8192\n\t"
  "s_waitcnt lgkmcnt(10)\n\t"
  "v_mfma_f32_16x16x32_bf16 v[0:3], v[228:231], v[196:199], v[0:3]\n\t"
  "ds_read_b64_tr_b16 v[242:243], v250 offset:8704\n\t"
  "s_waitcnt lgkmcnt(8)\n\t"
  "v_mfma_f32_16x16x32_bf16 v[24:27], v[228:231], v[204:207], v[24:27]\n\t"
  "ds_read_b64_tr_b16 v[244:245], v250 offset:12288\n\t"
  "v_mfma_f32_16x16x32_bf16 v[24:27], v[224:227], v[200:203], v[24:27]\n\t"
  "ds_read_b64_tr_b16 v[246:247], v250 offset:12800\n\t"
  "s_waitcnt lgkmcnt(4)\n\t"
  "v_mfma_f32_16x16x32_bf16 v[48:51], v[224:227], v[208:211], v[48:51]\n\t"
  "v_mfma_f32_16x16x32_bf16 v[48:51], v[228:231], v[212:215], v[48:51]\n\t"
  "v_mfma_f32_16x16x32_bf16 v[72:75], v[228:231], v[220:223], v[72:75]\n\t"
  "v_mfma_f32_16x16x32_bf16 v[72:75], v[224:227], v[216:219], v[72:75]\n\t"
  "v_mfma_f32_16x16x32_bf16 v[76:79], v[232:235], v[216:219], v[76:79]\n\t"
  "ds_read_b64_tr_b16 v[224:225], v251 offset:8192\n\t"
  "v_mfma_f32_16x16x32_bf16 v[76:79], v[236:239], v[220:223], v[76:79]\n\t"
  "ds_read_b64_tr_b16 v[226:227], v251 offset:8704\n\t"
  "v_mfma_f32_16x16x32_bf16 v[52:55], v[236:239], v[212:215], v[52:55]\n\t"
  "ds_read_b64_tr_b16 v[228:229], v251 offset:12288\n\t"
  "v_mfma_f32_16x16x32_bf16 v[52:55], v[232:235], v[208:211], v[52:55]\n\t"
  "ds_read_b64_tr_b16 v[230:231], v251 offset:12800\n\t"
  "v_mfma_f32_16x16x32_bf16 v[28:31], v[232:235], v[200:203], v[28:31]\n\t"
  "v_mfma_f32_16x16x32_bf16 v[28:31], v[236:239], v[204:207], v[28:31]\n\t"
  "v_mfma_f32_16x16x32_bf16 v[4:7], v[236:239], v[196:199], v[4:7]\n\t"
  "v_mfma_f32_16x16x32_bf16 v[4:7], v[232:235], v[192:195], v[4:7]\n\t"
  "s_waitcnt lgkmcnt(6)\n\t"
  "v_mfma_f32_16x16x32_bf16 v[8:11], v[240:243], v[192:195], v[8:11]\n\t"
  "ds_read_b64_tr_b16 v[232:233], v250 offset:16384\n\t"
  "s_waitcnt lgkmcnt(5)\n\t"
  "v_mfma_f32_16x16x32_bf16 v[8:11], v[244:247], v[196:199], v[8:11]\n\t"
  "ds_read_b64_tr_b16 v[234:235], v250 offset:16896\n\t"
  "v_mfma_f32_16x16x32_bf16 v[32:35], v[244:247], v[204:207], v[32:35]\n\t"
  "ds_read_b64_tr_b16 v[236:237], v250 offset:20480\n\t"
  "v_mfma_f32_16x16x32_bf16 v[32:35], v[240:243], v[200:203], v[32:35]\n\t"
  "ds_read_b64_tr_b16 v[238:239], v250 offset:20992\n\t"
  "v_mfma_f32_16x16x32_bf16 v[56:59], v[240:243], v[208:211], v[56:59]\n\t"
  "v_mfma_f32_16x16x32_bf16 v[56:59], v[244:247], v[212:215], v[56:59]\n\t"
  "v_mfma_f32_16x16x32_bf16 v[80:83], v[244:247], v[220:223], v[80:83]\n\t"
  "v_mfma_f32_16x16x32_bf16 v[80:83], v[240:243], v[216:219], v[80:83]\n\t"
  "s_waitcnt lgkmcnt(6)\n\t"
  "v_mfma_f32_16x16x32_bf16 v[84:87], v[224:227], v[216:219], v[84:87]\n\t"
  "ds_read_b64_tr_b16 v[240:241], v251 offset:16384\n\t"
  "s_waitcnt lgkmcnt(5)\n\t"
  "v_mfma_f32_16x16x32_bf16 v[84:87], v[228:231], v[220:223], v[84:87]\n\t"
  "ds_read_b64_tr_b16 v[242:243], v251 offset:16896\n\t"
  "v_mfma_f32_16x16x32_bf16 v[60:63], v[228:231], v[212:215], v[60:63]\n\t"
  "ds_read_b64_tr_b16 v[244:245], v251 offset:20480\n\t"
  "v_mfma_f32_16x16x32_bf16 v[60:63], v[224:227], v[208:211], v[60:63]\n\t"
  "ds_read_b64_tr_b16 v[246:247], v251 offset:20992\n\t"
  "v_mfma_f32_16x16x32_bf16 v[36:39], v[224:227], v[200:203], v[36:39]\n\t"
  "v_mfma_f32_16x16x32_bf16 v[36:39], v[228:231], v[204:207], v[36:39]\n\t"
  "v_mfma_f32_16x16x32_bf16 v[12:15], v[228:231], v[196:199], v[12:15]\n\t"
  "v_mfma_f32_16x16x32_bf16 v[12:15], v[224:227], v[192:195], v[12:15]\n\t"
  "s_waitcnt lgkmcnt(6)\n\t"
  "v_mfma_f32_16x16x32_bf16 v[16:19], v[192:195], v[232:235], v[16:19]\n\t"
  "ds_read_b64_tr_b16 v[224:225], v252 offset:0\n\t"
  "s_waitcnt lgkmcnt(5)\n\t"
  "v_mfma_f32_16x16x32_bf16 v[16:19], v[196:199], v[236:239], v[16:19]\n\t"
  "ds_read_b64_tr_b16 v[226:227], v252 offset:512\n\t"
  "v_mfma_f32_16x16x32_bf16 v[40:43], v[204:207], v[236:239], v[40:43]\n\t"
  "ds_read_b64_tr_b16 v[228:229], v252 offset:4096\n\t"
  "v_mfma_f32_16x16x32_bf16 v[40:43], v[200:203], v[232:235], v[40:43]\n\t"
  "ds_read_b64_tr_b16 v[230:231], v252 offset:4608\n\t"
  "v_mfma_f32_16x16x32_bf16 v[64:67], v[208:211], v[232:235], v[64:67]\n\t"
  "v_mfma_f32_16x16x32_bf16 v[64:67], v[212:215], v[236:239], v[64:67]\n\t"
  "v_mfma_f32_16x16x32_bf16 v[88:91], v[220:223], v[236:239], v[88:91]\n\t"
  "v_mfma_f32_16x16x32_bf16 v[88:91], v[216:219], v[232:235], v[88:91]\n\t"
  "s_waitcnt lgkmcnt(6)\n\t"
  "v_mfma_f32_16x16x32_bf16 v[92:95], v[216:219], v[240:243], v[92:95]\n\t"
  "ds_read_b64_tr_b16 v[232:233], v253 offset:0\n\t"
  "s_waitcnt lgkmcnt(5)\n\t"
  "v_mfma_f32_16x16x32_bf16 v[92:95], v[220:223], v[244:247], v[92:95]\n\t"
  "ds_read_b64_tr_b16 v[234:235], v253 offset:512\n\t"
  "v_mfma_f32_16x16x32_bf16 v[68:71], v[212:215], v[244:247], v[68:71]\n\t"
  "ds_read_b64_tr_b16 v[236:237], v253 offset:4096\n\t"
  "v_mfma_f32_16x16x32_bf16 v[68:71], v[208:211], v[240:243], v[68:71]\n\t"
  "ds_read_b64_tr_b16 v[238:239], v253 offset:4608\n\t"
  "v_mfma_f32_16x16x32_bf16 v[44:47], v[200:203], v[240:243], v[44:47]\n\t"
  "v_mfma_f32_16x16x32_bf16 v[44:47], v[204:207], v[244:247], v[44:47]\n\t"
  "v_mfma_f32_16x16x32_bf16 v[20:23], v[196:199], v[244:247], v[20:23]\n\t"
  "v_mfma_f32_16x16x32_bf16 v[20:23], v[192:195], v[240:243], v[20:23]\n\t"
  "s_waitcnt lgkmcnt(6)\n\t"
  "v_mfma_f32_16x16x32_bf16 v[96:99], v[224:227], v[192:195], v[96:99]\n\t"
  "ds_read_b64_tr_b16 v[240:241], v252 offset:8192\n\t"
  "s_waitcnt lgkmcnt(5)\n\t"
  "v_mfma_f32_16x16x32_bf16 v[96:99], v[228:231], v[196:199], v[96:99]\n\t"
  "ds_read_b64_tr_b16 v[242:243], v252 offset:8704\n\t"
  "v_mfma_f32_16x16x32_bf16 v[120:123], v[228:231], v[204:207], v[120:123]\n\t"
  "ds_read_b64_tr_b16 v[244:245], v252 offset:12288\n\t"
  "v_mfma_f32_16x16x32_bf16 v[120:123], v[224:227], v[200:203], v[120:123]\n\t"
  "ds_read_b64_tr_b16 v[246:247], v252 offset:12800\n\t"
  "v_mfma_f32_16x16x32_bf16 v[144:147], v[224:227], v[208:211], v[144:147]\n\t"
  "v_mfma_f32_16x16x32_bf16 v[144:147], v[228:231], v[212:215], v[144:147]\n\t"
  "v_mfma_f32_16x16x32_bf16 v[168:171], v[228:231], v[220:223], v[168:171]\n\t"
  "v_mfma_f32_16x16x32_bf16 v[168:171], v[224:227], v[216:219], v[168:171]\n\t"
  "s_waitcnt lgkmcnt(6)\n\t"
  "v_mfma_f32_16x16x32_bf16 v[172:175], v[232:235], v[216:219], v[172:175]\n\t"
  "ds_read_b64_tr_b16 v[224:225], v253 offset:8192\n\t"
  "s_waitcnt lgkmcnt(5)\n\t"
  "v_mfma_f32_16x16x32_bf16 v[172:175], v[236:239], v[220:223], v[172:175]\n\t"
  "ds_read_b64_tr_b16 v[226:227], v253 offset:8704\n\t"
  "v_mfma_f32_16x16x32_bf16 v[148:151], v[236:239], v[212:215], v[148:151]\n\t"
  "ds_read_b64_tr_b16 v[228:229], v253 offset:12288\n\t"
  "v_mfma_f32_16x16x32_bf16 v[148:151], v[232:235], v[208:211], v[148:151]\n\t"
  "ds_read_b64_tr_b16 v[230:231], v253 offset:12800\n\t"
  "v_mfma_f32_16x16x32_bf16 v[124:127], v[232:235], v[200:203], v[124:127]\n\t"
  "v_mfma_f32_16x16x32_bf16 v[124:127], v[236:239], v[204:207], v[124:127]\n\t"
  "v_mfma_f32_16x16x32_bf16 v[100:103], v[236:239], v[196:199], v[100:103]\n\t"
  "v_mfma_f32_16x16x32_bf16 v[100:103], v[232:235], v[192:195], v[100:103]\n\t"
  "s_waitcnt lgkmcnt(6)\n\t"
  "v_mfma_f32_16x16x32_bf16 v[104:107], v[240:243], v[192:195], v[104:107]\n\t"
  "ds_read_b64_tr_b16 v[232:233], v252 offset:16384\n\t"
  "s_waitcnt lgkmcnt(5)\n\t"
  "v_mfma_f32_16x16x32_bf16 v[104:107], v[244:247], v[196:199], v[104:107]\n\t"
  "ds_read_b64_tr_b16 v[234:235], v252 offset:16896\n\t"
  "v_mfma_f32_16x16x32_bf16 v[128:131], v[244:247], v[204:207], v[128:131]\n\t"
  "ds_read_b64_tr_b16 v[236:237], v252 offset:20480\n\t"
  "v_mfma_f32_16x16x32_bf16 v[128:131], v[240:243], v[200:203], v[128:131]\n\t"
  "ds_read_b64_tr_b16 v[238:239], v252 offset:20992\n\t"
  "v_mfma_f32_16x16x32_bf16 v[152:155], v[240:243], v[208:211], v[152:155]\n\t"
  "v_mfma_f32_16x16x32_bf16 v[152:155], v[244:247], v[212:215], v[152:155]\n\t"
  "v_mfma_f32_16x16x32_bf16 v[176:179], v[244:247], v[220:223], v[176:179]\n\t"
  "v_mfma_f32_16x16x32_bf16 v[176:179], v[240:243], v[216:219], v[176:179]\n\t"
  "s_waitcnt lgkmcnt(6)\n\t"
  "v_mfma_f32_16x16x32_bf16 v[180:183], v[224:227], v[216:219], v[180:183]\n\t"
  "ds_read_b64_tr_b16 v[240:241], v253 offset:16384\n\t"
  "s_waitcnt lgkmcnt(5)\n\t"
  "v_mfma_f32_16x16x32_bf16 v[180:183], v[228:231], v[220:223], v[180:183]\n\t"
  "ds_read_b64_tr_b16 v[242:243], v253 offset:16896\n\t"
  "v_mfma_f32_16x16x32_bf16 v[156:159], v[228:231], v[212:215], v[156:159]\n\t"
  "ds_read_b64_tr_b16 v[244:245], v253 offset:20480\n\t"
  "v_mfma_f32_16x16x32_bf16 v[156:159], v[224:227], v[208:211], v[156:159]\n\t"
  "ds_read_b64_tr_b16 v[246:247], v253 offset:20992\n\t"
  "v_mfma_f32_16x16x32_bf16 v[132:135], v[224:227], v[200:203], v[132:135]\n\t"
  "v_mfma_f32_16x16x32_bf16 v[132:135], v[228:231], v[204:207], v[132:135]\n\t"
  "v_mfma_f32_16x16x32_bf16 v[108:111], v[228:231], v[196:199], v[108:111]\n\t"
  "v_mfma_f32_16x16x32_bf16 v[108:111], v[224:227], v[192:195], v[108:111]\n\t"
  "s_waitcnt lgkmcnt(4)\n\t"
  "v_mfma_f32_16x16x32_bf16 v[112:115], v[192:195], v[232:235], v[112:115]\n\t"
  "v_mfma_f32_16x16x32_bf16 v[112:115], v[196:199], v[236:239], v[112:115]\n\t"
  "v_mfma_f32_16x16x32_bf16 v[136:139], v[204:207], v[236:239], v[136:139]\n\t"
  "v_mfma_f32_16x16x32_bf16 v[136:139], v[200:203], v[232:235], v[136:139]\n\t"
  "s_waitcnt lgkmcnt(0)\n\t"
  "s_barrier\n\t"
  "v_mfma_f32_16x16x32_bf16 v[160:163], v[208:211], v[232:235], v[160:163]\n\t"
  "ds_read_b64_tr_b16 v[224:225], v250 offset:24576\n\t"
  "v_mfma_f32_16x16x32_bf16 v[160:163], v[212:215], v[236:239], v[160:163]\n\t"
  "ds_read_b64_tr_b16 v[226:227], v250 offset:25088\n\t"
  "v_mfma_f32_16x16x32_bf16 v[184:187], v[220:223], v[236:239], v[184:187]\n\t"
  "ds_read_b64_tr_b16 v[228:229], v250 offset:28672\n\t"
  "v_mfma_f32_16x16x32_bf16 v[184:187], v[216:219], v[232:235], v[184:187]\n\t"
  "ds_read_b64_tr_b16 v[230:231], v250 offset:29184\n\t"
  "v_mfma_f32_16x16x32_bf16 v[116:119], v[192:195], v[240:243], v[116:119]\n\t"
  "ds_read_b64_tr_b16 v[232:233], v251 offset:24576\n\t"
  "v_mfma_f32_16x16x32_bf16 v[116:119], v[196:199], v[244:247], v[116:119]\n\t"
  "ds_read_b128 v[192:195], v248 offset:8192\n\t"
  "ds_read_b128 v[196:199], v249 offset:8192\n\t"
  "v_mfma_f32_16x16x32_bf16 v[140:143], v[204:207], v[244:247], v[140:143]\n\t"
  "ds_read_b64_tr_b16 v[234:235], v251 offset:25088\n\t"
  "v_mfma_f32_16x16x32_bf16 v[140:143], v[200:203], v[240:243], v[140:143]\n\t"
  "ds_read_b128 v[200:203], v248 offset:10240\n\t"
  "ds_read_b128 v[204:207], v249 offset:10240\n\t"
  "v_mfma_f32_16x16x32_bf16 v[164:167], v[208:211], v[240:243], v[164:167]\n\t"
  "ds_read_b64_tr_b16 v[236:237], v251 offset:28672\n\t"
  "v_mfma_f32_16x16x32_bf16 v[164:167], v[212:215], v[244:247], v[164:167]\n\t"
  "ds_read_b128 v[208:211], v248 offset:12288\n\t"
  "ds_read_b128 v[212:215], v249 offset:12288\n\t"
  "v_mfma_f32_16x16x32_bf16 v[188:191], v[220:223], v[244:247], v[188:191]\n\t"
  "ds_read_b64_tr_b16 v[238:239], v251 offset:29184\n\t"
  "v_mfma_f32_16x16x32_bf16 v[188:191], v[216:219], v[240:243], v[188:191]\n\t"
  "ds_read_b128 v[216:219], v248 offset:14336\n\t"
  "ds_read_b128 v[220:223], v249 offset:14336\n\t"
  "s_waitcnt lgkmcnt(10)\n\t"
  "v_mfma_f32_16x16x32_bf16 v[0:3], v[224:227], v[192:195], v[0:3]\n\t"
  "ds_read_b64_tr_b16 v[240:241], v250 offset:32768\n\t"
  "s_waitcnt lgkmcnt(10)\n\t"
  "v_mfma_f32_16x16x32_bf16 v[0:3], v[228:231], v[196:199], v[0:3]\n\t"
  "ds_read_b64_tr_b16 v[242:243], v250 offset:33280\n\t"
  "s_waitcnt lgkmcnt(8)\n\t"
  "v_mfma_f32_16x16x32_bf16 v[24:27], v[228:231], v[204:207], v[24:27]\n\t"
  "ds_read_b64_tr_b16 v[244:245], v250 offset:36864\n\t"
  "v_mfma_f32_16x16x32_bf16 v[24:27], v[224:227], v[200:203], v[24:27]\n\t"
  "ds_read_b64_tr_b16 v[246:247], v250 offset:37376\n\t"
  "s_waitcnt lgkmcnt(4)\n\t"
  "v_mfma_f32_16x16x32_bf16 v[48:51], v[224:227], v[208:211], v[48:51]\n\t"
  "v_mfma_f32_16x16x32_bf16 v[48:51], v[228:231], v[212:215], v[48:51]\n\t"
  "v_mfma_f32_16x16x32_bf16 v[72:75], v[228:231], v[220:223], v[72:75]\n\t"
  "v_mfma_f32_16x16x32_bf16 v[72:75], v[224:227], v[216:219], v[72:75]\n\t"
  "v_mfma_f32_16x16x32_bf16 v[76:79], v[232:235], v[216:219], v[76:79]\n\t"
  "ds_read_b64_tr_b16 v[224:225], v251 offset:32768\n\t"
  "v_mfma_f32_16x16x32_bf16 v[76:79], v[236:239], v[220:223], v[76:79]\n\t"
  "ds_read_b64_tr_b16 v[226:227], v251 offset:33280\n\t"
  "v_mfma_f32_16x16x32_bf16 v[52:55], v[236:239], v[212:215], v[52:55]\n\t"
  "ds_read_b64_tr_b16 v[228:229], v251 offset:36864\n\t"
  "v_mfma_f32_16x16x32_bf16 v[52:55], v[232:235], v[208:211], v[52:55]\n\t"
  "ds_read_b64_tr_b16 v[230:231], v251 offset:37376\n\t"
  "v_mfma_f32_16x16x32_bf16 v[28:31], v[232:235], v[200:203], v[28:31]\n\t"
  "v_mfma_f32_16x16x32_bf16 v[28:31], v[236:239], v[204:207], v[28:31]\n\t"
  "v_mfma_f32_16x16x32_bf16 v[4:7], v[236:239], v[196:199], v[4:7]\n\t"
  "v_mfma_f32_16x16x32_bf16 v[4:7], v[232:235], v[192:195], v[4:7]\n\t"
  "s_waitcnt lgkmcnt(6)\n\t"
  "v_mfma_f32_16x16x32_bf16 v[8:11], v[240:243], v[192:195], v[8:11]\n\t"
  "ds_read_b64_tr_b16 v[232:233], v250 offset:40960\n\t"
  "s_waitcnt lgkmcnt(5)\n\t"
  "v_mfma_f32_16x16x32_bf16 v[8:11], v[244:247], v[196:199], v[8:11]\n\t"
  "ds_read_b64_tr_b16 v[234:235], v250 offset:41472\n\t"
  "v_mfma_f32_16x16x32_bf16 v[32:35], v[244:247], v[204:207], v[32:35]\n\t"
  "ds_read_b64_tr_b16 v[236:237], v250 offset:45056\n\t"
  "v_mfma_f32_16x16x32_bf16 v[32:35], v[240:243], v[200:203], v[32:35]\n\t"
  "ds_read_b64_tr_b16 v[238:239], v250 offset:45568\n\t"
  "v_mfma_f32_16x16x32_bf16 v[56:59], v[240:243], v[208:211], v[56:59]\n\t"
  "v_mfma_f32_16x16x32_bf16 v[56:59], v[244:247], v[212:215], v[56:59]\n\t"
  "v_mfma_f32_16x16x32_bf16 v[80:83], v[244:247], v[220:223], v[80:83]\n\t"
  "v_mfma_f32_16x16x32_bf16 v[80:83], v[240:243], v[216:219], v[80:83]\n\t"
  "s_waitcnt lgkmcnt(6)\n\t"
  "v_mfma_f32_16x16x32_bf16 v[84:87], v[224:227], v[216:219], v[84:87]\n\t"
  "ds_read_b64_tr_b16 v[240:241], v251 offset:40960\n\t"
  "s_waitcnt lgkmcnt(5)\n\t"
  "v_mfma_f32_16x16x32_bf16 v[84:87], v[228:231], v[220:223], v[84:87]\n\t"
  "ds_read_b64_tr_b16 v[242:243], v251 offset:41472\n\t"
  "v_mfma_f32_16x16x32_bf16 v[60:63], v[228:231], v[212:215], v[60:63]\n\t"
  "ds_read_b64_tr_b16 v[244:245], v251 offset:45056\n\t"
  "v_mfma_f32_16x16x32_bf16 v[60:63], v[224:227], v[208:211], v[60:63]\n\t"
  "ds_read_b64_tr_b16 v[246:247], v251 offset:45568\n\t"
  "v_mfma_f32_16x16x32_bf16 v[36:39], v[224:227], v[200:203], v[36:39]\n\t"
  "v_mfma_f32_16x16x32_bf16 v[36:39], v[228:231], v[204:207], v[36:39]\n\t"
  "v_mfma_f32_16x16x32_bf16 v[12:15], v[228:231], v[196:199], v[12:15]\n\t"
  "v_mfma_f32_16x16x32_bf16 v[12:15], v[224:227], v[192:195], v[12:15]\n\t"
  "s_waitcnt lgkmcnt(6)\n\t"
  "v_mfma_f32_16x16x32_bf16 v[16:19], v[192:195], v[232:235], v[16:19]\n\t"
  "ds_read_b64_tr_b16 v[224:225], v252 offset:24576\n\t"
  "s_waitcnt lgkmcnt(5)\n\t"
  "v_mfma_f32_16x16x32_bf16 v[16:19], v[196:199], v[236:239], v[16:19]\n\t"
  "ds_read_b64_tr_b16 v[226:227], v252 offset:25088\n\t"
  "v_mfma_f32_16x16x32_bf16 v[40:43], v[204:207], v[236:239], v[40:43]\n\t"
  "ds_read_b64_tr_b16 v[228:229], v252 offset:28672\n\t"
  "v_mfma_f32_16x16x32_bf16 v[40:43], v[200:203], v[232:235], v[40:43]\n\t"
  "ds_read_b64_tr_b16 v[230:231], v252 offset:29184\n\t"
  "v_mfma_f32_16x16x32_bf16 v[64:67], v[208:211], v[232:235], v[64:67]\n\t"
  "v_mfma_f32_16x16x32_bf16 v[64:67], v[212:215], v[236:239], v[64:67]\n\t"
  "v_mfma_f32_16x16x32_bf16 v[88:91], v[220:223], v[236:239], v[88:91]\n\t"
  "v_mfma_f32_16x16x32_bf16 v[88:91], v[216:219], v[232:235], v[88:91]\n\t"
  "s_waitcnt lgkmcnt(6)\n\t"
  "v_mfma_f32_16x16x32_bf16 v[92:95], v[216:219], v[240:243], v[92:95]\n\t"
  "ds_read_b64_tr_b16 v[232:233], v253 offset:24576\n\t"
  "s_waitcnt lgkmcnt(5)\n\t"
  "v_mfma_f32_16x16x32_bf16 v[92:95], v[220:223], v[244:247], v[92:95]\n\t"
  "ds_read_b64_tr_b16 v[234:235], v253 offset:25088\n\t"
  "v_mfma_f32_16x16x32_bf16 v[68:71], v[212:215], v[244:247], v[68:71]\n\t"
  "ds_read_b64_tr_b16 v[236:237], v253 offset:28672\n\t"
  "v_mfma_f32_16x16x32_bf16 v[68:71], v[208:211], v[240:243], v[68:71]\n\t"
  "ds_read_b64_tr_b16 v[238:239], v253 offset:29184\n\t"
  "v_mfma_f32_16x16x32_bf16 v[44:47], v[200:203], v[240:243], v[44:47]\n\t"
  "v_mfma_f32_16x16x32_bf16 v[44:47], v[204:207], v[244:247], v[44:47]\n\t"
  "v_mfma_f32_16x16x32_bf16 v[20:23], v[196:199], v[244:247], v[20:23]\n\t"
  "v_mfma_f32_16x16x32_bf16 v[20:23], v[192:195], v[240:243], v[20:23]\n\t"
  "s_waitcnt lgkmcnt(6)\n\t"
  "v_mfma_f32_16x16x32_bf16 v[96:99], v[224:227], v[192:195], v[96:99]\n\t"
  "ds_read_b64_tr_b16 v[240:241], v252 offset:32768\n\t"
  "s_waitcnt lgkmcnt(5)\n\t"
  "v_mfma_f32_16x16x32_bf16 v[96:99], v[228:231], v[196:199], v[96:99]\n\t"
  "ds_read_b64_tr_b16 v[242:243], v252 offset:33280\n\t"
  "v_mfma_f32_16x16x32_bf16 v[120:123], v[228:231], v[204:207], v[120:123]\n\t"
  "ds_read_b64_tr_b16 v[244:245], v252 offset:36864\n\t"
  "v_mfma_f32_16x16x32_bf16 v[120:123], v[224:227], v[200:203], v[120:123]\n\t"
  "ds_read_b64_tr_b16 v[246:247], v252 offset:37376\n\t"
  "v_mfma_f32_16x16x32_bf16 v[144:147], v[224:227], v[208:211], v[144:147]\n\t"
  "v_mfma_f32_16x16x32_bf16 v[144:147], v[228:231], v[212:215], v[144:147]\n\t"
  "v_mfma_f32_16x16x32_bf16 v[168:171], v[228:231], v[220:223], v[168:171]\n\t"
  "v_mfma_f32_16x16x32_bf16 v[168:171], v[224:227], v[216:219], v[168:171]\n\t"
  "s_waitcnt lgkmcnt(6)\n\t"
  "v_mfma_f32_16x16x32_bf16 v[172:175], v[232:235], v[216:219], v[172:175]\n\t"
  "ds_read_b64_tr_b16 v[224:225], v253 offset:32768\n\t"
  "s_waitcnt lgkmcnt(5)\n\t"
  "v_mfma_f32_16x16x32_bf16 v[172:175], v[236:239], v[220:223], v[172:175]\n\t"
  "ds_read_b64_tr_b16 v[226:227], v253 offset:33280\n\t"
  "v_mfma_f32_16x16x32_bf16 v[148:151], v[236:239], v[212:215], v[148:151]\n\t"
  "ds_read_b64_tr_b16 v[228:229], v253 offset:36864\n\t"
  "v_mfma_f32_16x16x32_bf16 v[148:151], v[232:235], v[208:211], v[148:151]\n\t"
  "ds_read_b64_tr_b16 v[230:231], v253 offset:37376\n\t"
  "v_mfma_f32_16x16x32_bf16 v[124:127], v[232:235], v[200:203], v[124:127]\n\t"
  "v_mfma_f32_16x16x32_bf16 v[124:127], v[236:239], v[204:207], v[124:127]\n\t"
  "v_mfma_f32_16x16x32_bf16 v[100:103], v[236:239], v[196:199], v[100:103]\n\t"
  "v_mfma_f32_16x16x32_bf16 v[100:103], v[232:235], v[192:195], v[100:103]\n\t"
  "s_waitcnt lgkmcnt(6)\n\t"
  "v_mfma_f32_16x16x32_bf16 v[104:107], v[240:243], v[192:195], v[104:107]\n\t"
  "ds_read_b64_tr_b16 v[232:233], v252 offset:40960\n\t"
  "s_waitcnt lgkmcnt(5)\n\t"
  "v_mfma_f32_16x16x32_bf16 v[104:107], v[244:247], v[196:199], v[104:107]\n\t"
  "ds_read_b64_tr_b16 v[234:235], v252 offset:41472\n\t"
  "v_mfma_f32_16x16x32_bf16 v[128:131], v[244:247], v[204:207], v[128:131]\n\t"
  "ds_read_b64_tr_b16 v[236:237], v252 offset:45056\n\t"
  "v_mfma_f32_16x16x32_bf16 v[128:131], v[240:243], v[200:203], v[128:131]\n\t"
  "ds_read_b64_tr_b16 v[238:239], v252 offset:45568\n\t"
  "v_mfma_f32_16x16x32_bf16 v[152:155], v[240:243], v[208:211], v[152:155]\n\t"
  "v_mfma_f32_16x16x32_bf16 v[152:155], v[244:247], v[212:215], v[152:155]\n\t"
  "v_mfma_f32_16x16x32_bf16 v[176:179], v[244:247], v[220:223], v[176:179]\n\t"
  "v_mfma_f32_16x16x32_bf16 v[176:179], v[240:243], v[216:219], v[176:179]\n\t"
  "s_waitcnt lgkmcnt(6)\n\t"
  "v_mfma_f32_16x16x32_bf16 v[180:183], v[224:227], v[216:219], v[180:183]\n\t"
  "ds_read_b64_tr_b16 v[240:241], v253 offset:40960\n\t"
  "s_waitcnt lgkmcnt(5)\n\t"
  "v_mfma_f32_16x16x32_bf16 v[180:183], v[228:231], v[220:223], v[180:183]\n\t"
  "ds_read_b64_tr_b16 v[242:243], v253 offset:41472\n\t"
  "v_mfma_f32_16x16x32_bf16 v[156:159], v[228:231], v[212:215], v[156:159]\n\t"
  "ds_read_b64_tr_b16 v[244:245], v253 offset:45056\n\t"
  "v_mfma_f32_16x16x32_bf16 v[156:159], v[224:227], v[208:211], v[156:159]\n\t"
  "ds_read_b64_tr_b16 v[246:247], v253 offset:45568\n\t"
  "v_mfma_f32_16x16x32_bf16 v[132:135], v[224:227], v[200:203], v[132:135]\n\t"
  "v_mfma_f32_16x16x32_bf16 v[132:135], v[228:231], v[204:207], v[132:135]\n\t"
  "v_mfma_f32_16x16x32_bf16 v[108:111], v[228:231], v[196:199], v[108:111]\n\t"
  "v_mfma_f32_16x16x32_bf16 v[108:111], v[224:227], v[192:195], v[108:111]\n\t"
  "s_waitcnt lgkmcnt(4)\n\t"
  "v_mfma_f32_16x16x32_bf16 v[112:115], v[192:195], v[232:235], v[112:115]\n\t"
  "v_mfma_f32_16x16x32_bf16 v[112:115], v[196:199], v[236:239], v[112:115]\n\t"
  "v_mfma_f32_16x16x32_bf16 v[136:139], v[204:207], v[236:239], v[136:139]\n\t"
  "v_mfma_f32_16x16x32_bf16 v[136:139], v[200:203], v[232:235], v[136:139]\n\t"
  "s_waitcnt lgkmcnt(0)\n\t"
  "s_barrier\n\t"
  "v_mfma_f32_16x16x32_bf16 v[160:163], v[208:211], v[232:235], v[160:163]\n\t"
  "ds_read_b64_tr_b16 v[224:225], v250 offset:0\n\t"
  "v_mfma_f32_16x16x32_bf16 v[160:163], v[212:215], v[236:239], v[160:163]\n\t"
  "ds_read_b64_tr_b16 v[226:227], v250 offset:512\n\t"
  "v_mfma_f32_16x16x32_bf16 v[184:187], v[220:223], v[236:239], v[184:187]\n\t"
  "ds_read_b64_tr_b16 v[228:229], v250 offset:4096\n\t"
  "v_mfma_f32_16x16x32_bf16 v[184:187], v[216:219], v[232:235], v[184:187]\n\t"
  "ds_read_b64_tr_b16 v[230:231], v250 offset:4608\n\t"
  "v_mfma_f32_16x16x32_bf16 v[116:119], v[192:195], v[240:243], v[116:119]\n\t"
  "ds_read_b64_tr_b16 v[232:233], v251 offset:0\n\t"
  "v_mfma_f32_16x16x32_bf16 v[116:119], v[196:199], v[244:247], v[116:119]\n\t"
  "ds_read_b128 v[192:195], v248 offset:16384\n\t"
  "ds_read_b128 v[196:199], v249 offset:16384\n\t"
  "v_mfma_f32_16x16x32_bf16 v[140:143], v[204:207], v[244:247], v[140:143]\n\t"
  "ds_read_b64_tr_b16 v[234:235], v251 offset:512\n\t"
  "v_mfma_f32_16x16x32_bf16 v[140:143], v[200:203], v[240:243], v[140:143]\n\t"
  "ds_read_b128 v[200:203], v248 offset:18432\n\t"
  "ds_read_b128 v[204:207], v249 offset:18432\n\t"
  "v_mfma_f32_16x16x32_bf16 v[164:167], v[208:211], v[240:243], v[164:167]\n\t"
  "ds_read_b64_tr_b16 v[236:237], v251 offset:4096\n\t"
  "v_mfma_f32_16x16x32_bf16 v[164:167], v[212:215], v[244:247], v[164:167]\n\t"
  "ds_read_b128 v[208:211], v248 offset:20480\n\t"
  "ds_read_b128 v[212:215], v249 offset:20480\n\t"
  "v_mfma_f32_16x16x32_bf16 v[188:191], v[220:223], v[244:247], v[188:191]\n\t"
  "ds_read_b64_tr_b16 v[238:239], v251 offset:4608\n\t"
  "v_mfma_f32_16x16x32_bf16 v[188:191], v[216:219], v[240:243], v[188:191]\n\t"
  "ds_read_b128 v[216:219], v248 offset:22528\n\t"
  "ds_read_b128 v[220:223], v249 offset:22528\n\t"
  "s_waitcnt lgkmcnt(10)\n\t"
  "v_mfma_f32_16x16x32_bf16 v[0:3], v[224:227], v[192:195], v[0:3]\n\t"
  "ds_read_b64_tr_b16 v[240:241], v250 offset:8192\n\t"
  "s_waitcnt lgkmcnt(10)\n\t"
  "v_mfma_f32_16x16x32_bf16 v[0:3], v[228:231], v[196:199], v[0:3]\n\t"
  "ds_read_b64_tr_b16 v[242:243], v250 offset:8704\n\t"
  "s_waitcnt lgkmcnt(8)\n\t"
  "v_mfma_f32_16x16x32_bf16 v[24:27], v[228:231], v[204:207], v[24:27]\n\t"
  "ds_read_b64_tr_b16 v[244:245], v250 offset:12288\n\t"
  "v_mfma_f32_16x16x32_bf16 v[24:27], v[224:227], v[200:203], v[24:27]\n\t"
  "ds_read_b64_tr_b16 v[246:247], v250 offset:12800\n\t"
  "s_waitcnt lgkmcnt(4)\n\t"
  "v_mfma_f32_16x16x32_bf16 v[48:51], v[224:227], v[208:211], v[48:51]\n\t"
  "v_mfma_f32_16x16x32_bf16 v[48:51], v[228:231], v[212:215], v[48:51]\n\t"
  "v_mfma_f32_16x16x32_bf16 v[72:75], v[228:231], v[220:223], v[72:75]\n\t"
  "v_mfma_f32_16x16x32_bf16 v[72:75], v[224:227], v[216:219], v[72:75]\n\t"
  "v_mfma_f32_16x16x32_bf16 v[76:79], v[232:235], v[216:219], v[76:79]\n\t"
  "ds_read_b64_tr_b16 v[224:225], v251 offset:8192\n\t"
  "v_mfma_f32_16x16x32_bf16 v[76:79], v[236:239], v[220:223], v[76:79]\n\t"
  "ds_read_b64_tr_b16 v[226:227], v251 offset:8704\n\t"
  "v_mfma_f32_16x16x32_bf16 v[52:55], v[236:239], v[212:215], v[52:55]\n\t"
  "ds_read_b64_tr_b16 v[228:229], v251 offset:12288\n\t"
  "v_mfma_f32_16x16x32_bf16 v[52:55], v[232:235], v[208:211], v[52:55]\n\t"
  "ds_read_b64_tr_b16 v[230:231], v251 offset:12800\n\t"
  "v_mfma_f32_16x16x32_bf16 v[28:31], v[232:235], v[200:203], v[28:31]\n\t"
  "v_mfma_f32_16x16x32_bf16 v[28:31], v[236:239], v[204:207], v[28:31]\n\t"
  "v_mfma_f32_16x16x32_bf16 v[4:7], v[236:239], v[196:199], v[4:7]\n\t"
  "v_mfma_f32_16x16x32_bf16 v[4:7], v[232:235], v[192:195], v[4:7]\n\t"
  "s_waitcnt lgkmcnt(6)\n\t"
  "v_mfma_f32_16x16x32_bf16 v[8:11], v[240:243], v[192:195], v[8:11]\n\t"
  "ds_read_b64_tr_b16 v[232:233], v250 offset:16384\n\t"
  "s_waitcnt lgkmcnt(5)\n\t"
  "v_mfma_f32_16x16x32_bf16 v[8:11], v[244:247], v[196:199], v[8:11]\n\t"
  "ds_read_b64_tr_b16 v[234:235], v250 offset:16896\n\t"
  "v_mfma_f32_16x16x32_bf16 v[32:35], v[244:247], v[204:207], v[32:35]\n\t"
  "ds_read_b64_tr_b16 v[236:237], v250 offset:20480\n\t"
  "v_mfma_f32_16x16x32_bf16 v[32:35], v[240:243], v[200:203], v[32:35]\n\t"
  "ds_read_b64_tr_b16 v[238:239], v250 offset:20992\n\t"
  "v_mfma_f32_16x16x32_bf16 v[56:59], v[240:243], v[208:211], v[56:59]\n\t"
  "v_mfma_f32_16x16x32_bf16 v[56:59], v[244:247], v[212:215], v[56:59]\n\t"
  "v_mfma_f32_16x16x32_bf16 v[80:83], v[244:247], v[220:223], v[80:83]\n\t"
  "v_mfma_f32_16x16x32_bf16 v[80:83], v[240:243], v[216:219], v[80:83]\n\t"
  "s_waitcnt lgkmcnt(6)\n\t"
  "v_mfma_f32_16x16x32_bf16 v[84:87], v[224:227], v[216:219], v[84:87]\n\t"
  "ds_read_b64_tr_b16 v[240:241], v251 offset:16384\n\t"
  "s_waitcnt lgkmcnt(5)\n\t"
  "v_mfma_f32_16x16x32_bf16 v[84:87], v[228:231], v[220:223], v[84:87]\n\t"
  "ds_read_b64_tr_b16 v[242:243], v251 offset:16896\n\t"
  "v_mfma_f32_16x16x32_bf16 v[60:63], v[228:231], v[212:215], v[60:63]\n\t"
  "ds_read_b64_tr_b16 v[244:245], v251 offset:20480\n\t"
  "v_mfma_f32_16x16x32_bf16 v[60:63], v[224:227], v[208:211], v[60:63]\n\t"
  "ds_read_b64_tr_b16 v[246:247], v251 offset:20992\n\t"
  "v_mfma_f32_16x16x32_bf16 v[36:39], v[224:227], v[200:203], v[36:39]\n\t"
  "v_mfma_f32_16x16x32_bf16 v[36:39], v[228:231], v[204:207], v[36:39]\n\t"
  "v_mfma_f32_16x16x32_bf16 v[12:15], v[228:231], v[196:199], v[12:15]\n\t"
  "v_mfma_f32_16x16x32_bf16 v[12:15], v[224:227], v[192:195], v[12:15]\n\t"
  "s_waitcnt lgkmcnt(6)\n\t"
  "v_mfma_f32_16x16x32_bf16 v[16:19], v[192:195], v[232:235], v[16:19]\n\t"
  "ds_read_b64_tr_b16 v[224:225], v252 offset:0\n\t"
  "s_waitcnt lgkmcnt(5)\n\t"
  "v_mfma_f32_16x16x32_bf16 v[16:19], v[196:199], v[236:239], v[16:19]\n\t"
  "ds_read_b64_tr_b16 v[226:227], v252 offset:512\n\t"
  "v_mfma_f32_16x16x32_bf16 v[40:43], v[204:207], v[236:239], v[40:43]\n\t"
  "ds_read_b64_tr_b16 v[228:229], v252 offset:4096\n\t"
  "v_mfma_f32_16x16x32_bf16 v[40:43], v[200:203], v[232:235], v[40:43]\n\t"
  "ds_read_b64_tr_b16 v[230:231], v252 offset:4608\n\t"
  "v_mfma_f32_16x16x32_bf16 v[64:67], v[208:211], v[232:235], v[64:67]\n\t"
  "v_mfma_f32_16x16x32_bf16 v[64:67], v[212:215], v[236:239], v[64:67]\n\t"
  "v_mfma_f32_16x16x32_bf16 v[88:91], v[220:223], v[236:239], v[88:91]\n\t"
  "v_mfma_f32_16x16x32_bf16 v[88:91], v[216:219], v[232:235], v[88:91]\n\t"
  "s_waitcnt lgkmcnt(6)\n\t"
  "v_mfma_f32_16x16x32_bf16 v[92:95], v[216:219], v[240:243], v[92:95]\n\t"
  "ds_read_b64_tr_b16 v[232:233], v253 offset:0\n\t"
  "s_waitcnt lgkmcnt(5)\n\t"
  "v_mfma_f32_16x16x32_bf16 v[92:95], v[220:223], v[244:247], v[92:95]\n\t"
  "ds_read_b64_tr_b16 v[234:235], v253 offset:512\n\t"
  "v_mfma_f32_16x16x32_bf16 v[68:71], v[212:215], v[244:247], v[68:71]\n\t"
  "ds_read_b64_tr_b16 v[236:237], v253 offset:4096\n\t"
  "v_mfma_f32_16x16x32_bf16 v[68:71], v[208:211], v[240:243], v[68:71]\n\t"
  "ds_read_b64_tr_b16 v[238:239], v253 offset:4608\n\t"
  "v_mfma_f32_16x16x32_bf16 v[44:47], v[200:203], v[240:243], v[44:47]\n\t"
  "v_mfma_f32_16x16x32_bf16 v[44:47], v[204:207], v[244:247], v[44:47]\n\t"
  "v_mfma_f32_16x16x32_bf16 v[20:23], v[196:199], v[244:247], v[20:23]\n\t"
  "v_mfma_f32_16x16x32_bf16 v[20:23], v[192:195], v[240:243], v[20:23]\n\t"
  "s_waitcnt lgkmcnt(6)\n\t"
  "v_mfma_f32_16x16x32_bf16 v[96:99], v[224:227], v[192:195], v[96:99]\n\t"
  "ds_read_b64_tr_b16 v[240:241], v252 offset:8192\n\t"
  "s_waitcnt lgkmcnt(5)\n\t"
  "v_mfma_f32_16x16x32_bf16 v[96:99], v[228:231], v[196:199], v[96:99]\n\t"
  "ds_read_b64_tr_b16 v[242:243], v252 offset:8704\n\t"
  "v_mfma_f32_16x16x32_bf16 v[120:123], v[228:231], v[204:207], v[120:123]\n\t"
  "ds_read_b64_tr_b16 v[244:245], v252 offset:12288\n\t"
  "v_mfma_f32_16x16x32_bf16 v[120:123], v[224:227], v[200:203], v[120:123]\n\t"
  "ds_read_b64_tr_b16 v[246:247], v252 offset:12800\n\t"
  "v_mfma_f32_16x16x32_bf16 v[144:147], v[224:227], v[208:211], v[144:147]\n\t"
  "v_mfma_f32_16x16x32_bf16 v[144:147], v[228:231], v[212:215], v[144:147]\n\t"
  "v_mfma_f32_16x16x32_bf16 v[168:171], v[228:231], v[220:223], v[168:171]\n\t"
  "v_mfma_f32_16x16x32_bf16 v[168:171], v[224:227], v[216:219], v[168:171]\n\t"
  "s_waitcnt lgkmcnt(6)\n\t"
  "v_mfma_f32_16x16x32_bf16 v[172:175], v[232:235], v[216:219], v[172:175]\n\t"
  "ds_read_b64_tr_b16 v[224:225], v253 offset:8192\n\t"
  "s_waitcnt lgkmcnt(5)\n\t"
  "v_mfma_f32_16x16x32_bf16 v[172:175], v[236:239], v[220:223], v[172:175]\n\t"
  "ds_read_b64_tr_b16 v[226:227], v253 offset:8704\n\t"
  "v_mfma_f32_16x16x32_bf16 v[148:151], v[236:239], v[212:215], v[148:151]\n\t"
  "ds_read_b64_tr_b16 v[228:229], v253 offset:12288\n\t"
  "v_mfma_f32_16x16x32_bf16 v[148:151], v[232:235], v[208:211], v[148:151]\n\t"
  "ds_read_b64_tr_b16 v[230:231], v253 offset:12800\n\t"
  "v_mfma_f32_16x16x32_bf16 v[124:127], v[232:235], v[200:203], v[124:127]\n\t"
  "v_mfma_f32_16x16x32_bf16 v[124:127], v[236:239], v[204:207], v[124:127]\n\t"
  "v_mfma_f32_16x16x32_bf16 v[100:103], v[236:239], v[196:199], v[100:103]\n\t"
  "v_mfma_f32_16x16x32_bf16 v[100:103], v[232:235], v[192:195], v[100:103]\n\t"
  "s_waitcnt lgkmcnt(6)\n\t"
  "v_mfma_f32_16x16x32_bf16 v[104:107], v[240:243], v[192:195], v[104:107]\n\t"
  "ds_read_b64_tr_b16 v[232:233], v252 offset:16384\n\t"
  "s_waitcnt lgkmcnt(5)\n\t"
  "v_mfma_f32_16x16x32_bf16 v[104:107], v[244:247], v[196:199], v[104:107]\n\t"
  "ds_read_b64_tr_b16 v[234:235], v252 offset:16896\n\t"
  "v_mfma_f32_16x16x32_bf16 v[128:131], v[244:247], v[204:207], v[128:131]\n\t"
  "ds_read_b64_tr_b16 v[236:237], v252 offset:20480\n\t"
  "v_mfma_f32_16x16x32_bf16 v[128:131], v[240:243], v[200:203], v[128:131]\n\t"
  "ds_read_b64_tr_b16 v[238:239], v252 offset:20992\n\t"
  "v_mfma_f32_16x16x32_bf16 v[152:155], v[240:243], v[208:211], v[152:155]\n\t"
  "v_mfma_f32_16x16x32_bf16 v[152:155], v[244:247], v[212:215], v[152:155]\n\t"
  "v_mfma_f32_16x16x32_bf16 v[176:179], v[244:247], v[220:223], v[176:179]\n\t"
  "v_mfma_f32_16x16x32_bf16 v[176:179], v[240:243], v[216:219], v[176:179]\n\t"
  "s_waitcnt lgkmcnt(6)\n\t"
  "v_mfma_f32_16x16x32_bf16 v[180:183], v[224:227], v[216:219], v[180:183]\n\t"
  "ds_read_b64_tr_b16 v[240:241], v253 offset:16384\n\t"
  "s_waitcnt lgkmcnt(5)\n\t"
  "v_mfma_f32_16x16x32_bf16 v[180:183], v[228:231], v[220:223], v[180:183]\n\t"
  "ds_read_b64_tr_b16 v[242:243], v253 offset:16896\n\t"
  "v_mfma_f32_16x16x32_bf16 v[156:159], v[228:231], v[212:215], v[156:159]\n\t"
  "ds_read_b64_tr_b16 v[244:245], v253 offset:20480\n\t"
  "v_mfma_f32_16x16x32_bf16 v[156:159], v[224:227], v[208:211], v[156:159]\n\t"
  "ds_read_b64_tr_b16 v[246:247], v253 offset:20992\n\t"
  "v_mfma_f32_16x16x32_bf16 v[132:135], v[224:227], v[200:203], v[132:135]\n\t"
  "v_mfma_f32_16x16x32_bf16 v[132:135], v[228:231], v[204:207], v[132:135]\n\t"
  "v_mfma_f32_16x16x32_bf16 v[108:111], v[228:231], v[196:199], v[108:111]\n\t"
  "v_mfma_f32_16x16x32_bf16 v[108:111], v[224:227], v[192:195], v[108:111]\n\t"
  "s_waitcnt lgkmcnt(4)\n\t"
  "v_mfma_f32_16x16x32_bf16 v[112:115], v[192:195], v[232:235], v[112:115]\n\t"
  "v_mfma_f32_16x16x32_bf16 v[112:115], v[196:199], v[236:239], v[112:115]\n\t"
  "v_mfma_f32_16x16x32_bf16 v[136:139], v[204:207], v[236:239], v[136:139]\n\t"
  "v_mfma_f32_16x16x32_bf16 v[136:139], v[200:203], v[232:235], v[136:139]\n\t"
  "s_waitcnt lgkmcnt(0)\n\t"
  "s_barrier\n\t"
  "v_mfma_f32_16x16x32_bf16 v[160:163], v[208:211], v[232:235], v[160:163]\n\t"
  "ds_read_b64_tr_b16 v[224:225], v250 offset:24576\n\t"
  "v_mfma_f32_16x16x32_bf16 v[160:163], v[212:215], v[236:239], v[160:163]\n\t"
  "ds_read_b64_tr_b16 v[226:227], v250 offset:25088\n\t"
  "v_mfma_f32_16x16x32_bf16 v[184:187], v[220:223], v[236:239], v[184:187]\n\t"
  "ds_read_b64_tr_b16 v[228:229], v250 offset:28672\n\t"
  "v_mfma_f32_16x16x32_bf16 v[184:187], v[216:219], v[232:235], v[184:187]\n\t"
  "ds_read_b64_tr_b16 v[230:231], v250 offset:29184\n\t"
  "v_mfma_f32_16x16x32_bf16 v[116:119], v[192:195], v[240:243], v[116:119]\n\t"
  "ds_read_b64_tr_b16 v[232:233], v251 offset:24576\n\t"
  "v_mfma_f32_16x16x32_bf16 v[116:119], v[196:199], v[244:247], v[116:119]\n\t"
  "ds_read_b128 v[192:195], v248 offset:0\n\t"
  "ds_read_b128 v[196:199], v249 offset:0\n\t"
  "v_mfma_f32_16x16x32_bf16 v[140:143], v[204:207], v[244:247], v[140:143]\n\t"
  "ds_read_b64_tr_b16 v[234:235], v251 offset:25088\n\t"
  "v_mfma_f32_16x16x32_bf16 v[140:143], v[200:203], v[240:243], v[140:143]\n\t"
  "ds_read_b128 v[200:203], v248 offset:2048\n\t"
  "ds_read_b128 v[204:207], v249 offset:2048\n\t"
  "v_mfma_f32_16x16x32_bf16 v[164:167], v[208:211], v[240:243], v[164:167]\n\t"
  "ds_read_b64_tr_b16 v[236:237], v251 offset:28672\n\t"
  "v_mfma_f32_16x16x32_bf16 v[164:167], v[212:215], v[244:247], v[164:167]\n\t"
  "ds_read_b128 v[208:211], v248 offset:4096\n\t"
  "ds_read_b128 v[212:215], v249 offset:4096\n\t"
  "v_mfma_f32_16x16x32_bf16 v[188:191], v[220:223], v[244:247], v[188:191]\n\t"
  "ds_read_b64_tr_b16 v[238:239], v251 offset:29184\n\t"
  "v_mfma_f32_16x16x32_bf16 v[188:191], v[216:219], v[240:243], v[188:191]\n\t"
  "ds_read_b128 v[216:219], v248 offset:6144\n\t"
  "ds_read_b128 v[220:223], v249 offset:6144\n\t"
  "s_waitcnt lgkmcnt(10)\n\t"
  "v_mfma_f32_16x16x32_bf16 v[0:3], v[224:227], v[192:195], v[0:3]\n\t"
  "ds_read_b64_tr_b16 v[240:241], v250 offset:32768\n\t"
  "s_waitcnt lgkmcnt(10)\n\t"
  "v_mfma_f32_16x16x32_bf16 v[0:3], v[228:231], v[196:199], v[0:3]\n\t"
  "ds_read_b64_tr_b16 v[242:243], v250 offset:33280\n\t"
  "s_waitcnt lgkmcnt(8)\n\t"
  "v_mfma_f32_16x16x32_bf16 v[24:27], v[228:231], v[204:207], v[24:27]\n\t"
  "ds_read_b64_tr_b16 v[244:245], v250 offset:36864\n\t"
  "v_mfma_f32_16x16x32_bf16 v[24:27], v[224:227], v[200:203], v[24:27]\n\t"
  "ds_read_b64_tr_b16 v[246:247], v250 offset:37376\n\t"
  "s_waitcnt lgkmcnt(4)\n\t"
  "v_mfma_f32_16x16x32_bf16 v[48:51], v[224:227], v[208:211], v[48:51]\n\t"
  "v_mfma_f32_16x16x32_bf16 v[48:51], v[228:231], v[212:215], v[48:51]\n\t"
  "v_mfma_f32_16x16x32_bf16 v[72:75], v[228:231], v[220:223], v[72:75]\n\t"
  "v_mfma_f32_16x16x32_bf16 v[72:75], v[224:227], v[216:219], v[72:75]\n\t"
  "v_mfma_f32_16x16x32_bf16 v[76:79], v[232:235], v[216:219], v[76:79]\n\t"
  "ds_read_b64_tr_b16 v[224:225], v251 offset:32768\n\t"
  "v_mfma_f32_16x16x32_bf16 v[76:79], v[236:239], v[220:223], v[76:79]\n\t"
  "ds_read_b64_tr_b16 v[226:227], v251 offset:33280\n\t"
  "v_mfma_f32_16x16x32_bf16 v[52:55], v[236:239], v[212:215], v[52:55]\n\t"
  "ds_read_b64_tr_b16 v[228:229], v251 offset:36864\n\t"
  "v_mfma_f32_16x16x32_bf16 v[52:55], v[232:235], v[208:211], v[52:55]\n\t"
  "ds_read_b64_tr_b16 v[230:231], v251 offset:37376\n\t"
  "v_mfma_f32_16x16x32_bf16 v[28:31], v[232:235], v[200:203], v[28:31]\n\t"
  "v_mfma_f32_16x16x32_bf16 v[28:31], v[236:239], v[204:207], v[28:31]\n\t"
  "v_mfma_f32_16x16x32_bf16 v[4:7], v[236:239], v[196:199], v[4:7]\n\t"
  "v_mfma_f32_16x16x32_bf16 v[4:7], v[232:235], v[192:195], v[4:7]\n\t"
  "s_waitcnt lgkmcnt(6)\n\t"
  "v_mfma_f32_16x16x32_bf16 v[8:11], v[240:243], v[192:195], v[8:11]\n\t"
  "ds_read_b64_tr_b16 v[232:233], v250 offset:40960\n\t"
  "s_waitcnt lgkmcnt(5)\n\t"
  "v_mfma_f32_16x16x32_bf16 v[8:11], v[244:247], v[196:199], v[8:11]\n\t"
  "ds_read_b64_tr_b16 v[234:235], v250 offset:41472\n\t"
  "v_mfma_f32_16x16x32_bf16 v[32:35], v[244:247], v[204:207], v[32:35]\n\t"
  "ds_read_b64_tr_b16 v[236:237], v250 offset:45056\n\t"
  "v_mfma_f32_16x16x32_bf16 v[32:35], v[240:243], v[200:203], v[32:35]\n\t"
  "ds_read_b64_tr_b16 v[238:239], v250 offset:45568\n\t"
  "v_mfma_f32_16x16x32_bf16 v[56:59], v[240:243], v[208:211], v[56:59]\n\t"
  "v_mfma_f32_16x16x32_bf16 v[56:59], v[244:247], v[212:215], v[56:59]\n\t"
  "v_mfma_f32_16x16x32_bf16 v[80:83], v[244:247], v[220:223], v[80:83]\n\t"
  "v_mfma_f32_16x16x32_bf16 v[80:83], v[240:243], v[216:219], v[80:83]\n\t"
  "s_waitcnt lgkmcnt(6)\n\t"
  "v_mfma_f32_16x16x32_bf16 v[84:87], v[224:227], v[216:219], v[84:87]\n\t"
  "ds_read_b64_tr_b16 v[240:241], v251 offset:40960\n\t"
  "s_waitcnt lgkmcnt(5)\n\t"
  "v_mfma_f32_16x16x32_bf16 v[84:87], v[228:231], v[220:223], v[84:87]\n\t"
  "ds_read_b64_tr_b16 v[242:243], v251 offset:41472\n\t"
  "v_mfma_f32_16x16x32_bf16 v[60:63], v[228:231], v[212:215], v[60:63]\n\t"
  "ds_read_b64_tr_b16 v[244:245], v251 offset:45056\n\t"
  "v_mfma_f32_16x16x32_bf16 v[60:63], v[224:227], v[208:211], v[60:63]\n\t"
  "ds_read_b64_tr_b16 v[246:247], v251 offset:45568\n\t"
  "v_mfma_f32_16x16x32_bf16 v[36:39], v[224:227], v[200:203], v[36:39]\n\t"
  "v_mfma_f32_16x16x32_bf16 v[36:39], v[228:231], v[204:207], v[36:39]\n\t"
  "v_mfma_f32_16x16x32_bf16 v[12:15], v[228:231], v[196:199], v[12:15]\n\t"
  "v_mfma_f32_16x16x32_bf16 v[12:15], v[224:227], v[192:195], v[12:15]\n\t"
  "s_waitcnt lgkmcnt(6)\n\t"
  "v_mfma_f32_16x16x32_bf16 v[16:19], v[192:195], v[232:235], v[16:19]\n\t"
  "ds_read_b64_tr_b16 v[224:225], v252 offset:24576\n\t"
  "s_waitcnt lgkmcnt(5)\n\t"
  "v_mfma_f32_16x16x32_bf16 v[16:19], v[196:199], v[236:239], v[16:19]\n\t"
  "ds_read_b64_tr_b16 v[226:227], v252 offset:25088\n\t"
  "v_mfma_f32_16x16x32_bf16 v[40:43], v[204:207], v[236:239], v[40:43]\n\t"
  "ds_read_b64_tr_b16 v[228:229], v252 offset:28672\n\t"
  "v_mfma_f32_16x16x32_bf16 v[40:43], v[200:203], v[232:235], v[40:43]\n\t"
  "ds_read_b64_tr_b16 v[230:231], v252 offset:29184\n\t"
  "v_mfma_f32_16x16x32_bf16 v[64:67], v[208:211], v[232:235], v[64:67]\n\t"
  "v_mfma_f32_16x16x32_bf16 v[64:67], v[212:215], v[236:239], v[64:67]\n\t"
  "v_mfma_f32_16x16x32_bf16 v[88:91], v[220:223], v[236:239], v[88:91]\n\t"
  "v_mfma_f32_16x16x32_bf16 v[88:91], v[216:219], v[232:235], v[88:91]\n\t"
  "s_waitcnt lgkmcnt(6)\n\t"
  "v_mfma_f32_16x16x32_bf16 v[92:95], v[216:219], v[240:243], v[92:95]\n\t"
  "ds_read_b64_tr_b16 v[232:233], v253 offset:24576\n\t"
  "s_waitcnt lgkmcnt(5)\n\t"
  "v_mfma_f32_16x16x32_bf16 v[92:95], v[220:223], v[244:247], v[92:95]\n\t"
  "ds_read_b64_tr_b16 v[234:235], v253 offset:25088\n\t"
  "v_mfma_f32_16x16x32_bf16 v[68:71], v[212:215], v[244:247], v[68:71]\n\t"
  "ds_read_b64_tr_b16 v[236:237], v253 offset:28672\n\t"
  "v_mfma_f32_16x16x32_bf16 v[68:71], v[208:211], v[240:243], v[68:71]\n\t"
  "ds_read_b64_tr_b16 v[238:239], v253 offset:29184\n\t"
  "v_mfma_f32_16x16x32_bf16 v[44:47], v[200:203], v[240:243], v[44:47]\n\t"
  "v_mfma_f32_16x16x32_bf16 v[44:47], v[204:207], v[244:247], v[44:47]\n\t"
  "v_mfma_f32_16x16x32_bf16 v[20:23], v[196:199], v[244:247], v[20:23]\n\t"
  "v_mfma_f32_16x16x32_bf16 v[20:23], v[192:195], v[240:243], v[20:23]\n\t"
  "s_waitcnt lgkmcnt(6)\n\t"
  "v_mfma_f32_16x16x32_bf16 v[96:99], v[224:227], v[192:195], v[96:99]\n\t"
  "ds_read_b64_tr_b16 v[240:241], v252 offset:32768\n\t"
  "s_waitcnt lgkmcnt(5)\n\t"
  "v_mfma_f32_16x16x32_bf16 v[96:99], v[228:231], v[196:199], v[96:99]\n\t"
  "ds_read_b64_tr_b16 v[242:243], v252 offset:33280\n\t"
  "v_mfma_f32_16x16x32_bf16 v[120:123], v[228:231], v[204:207], v[120:123]\n\t"
  "ds_read_b64_tr_b16 v[244:245], v252 offset:36864\n\t"
  "v_mfma_f32_16x16x32_bf16 v[120:123], v[224:227], v[200:203], v[120:123]\n\t"
  "ds_read_b64_tr_b16 v[246:247], v252 offset:37376\n\t"
  "v_mfma_f32_16x16x32_bf16 v[144:147], v[224:227], v[208:211], v[144:147]\n\t"
  "v_mfma_f32_16x16x32_bf16 v[144:147], v[228:231], v[212:215], v[144:147]\n\t"
  "v_mfma_f32_16x16x32_bf16 v[168:171], v[228:231], v[220:223], v[168:171]\n\t"
  "v_mfma_f32_16x16x32_bf16 v[168:171], v[224:227], v[216:219], v[168:171]\n\t"
  "s_waitcnt lgkmcnt(6)\n\t"
  "v_mfma_f32_16x16x32_bf16 v[172:175], v[232:235], v[216:219], v[172:175]\n\t"
  "ds_read_b64_tr_b16 v[224:225], v253 offset:32768\n\t"
  "s_waitcnt lgkmcnt(5)\n\t"
  "v_mfma_f32_16x16x32_bf16 v[172:175], v[236:239], v[220:223], v[172:175]\n\t"
  "ds_read_b64_tr_b16 v[226:227], v253 offset:33280\n\t"
  "v_mfma_f32_16x16x32_bf16 v[148:151], v[236:239], v[212:215], v[148:151]\n\t"
  "ds_read_b64_tr_b16 v[228:229], v253 offset:36864\n\t"
  "v_mfma_f32_16x16x32_bf16 v[148:151], v[232:235], v[208:211], v[148:151]\n\t"
  "ds_read_b64_tr_b16 v[230:231], v253 offset:37376\n\t"
  "v_mfma_f32_16x16x32_bf16 v[124:127], v[232:235], v[200:203], v[124:127]\n\t"
  "v_mfma_f32_16x16x32_bf16 v[124:127], v[236:239], v[204:207], v[124:127]\n\t"
  "v_mfma_f32_16x16x32_bf16 v[100:103], v[236:239], v[196:199], v[100:103]\n\t"
  "v_mfma_f32_16x16x32_bf16 v[100:103], v[232:235], v[192:195], v[100:103]\n\t"
  "s_waitcnt lgkmcnt(6)\n\t"
  "v_mfma_f32_16x16x32_bf16 v[104:107], v[240:243], v[192:195], v[104:107]\n\t"
  "ds_read_b64_tr_b16 v[232:233], v252 offset:40960\n\t"
  "s_waitcnt lgkmcnt(5)\n\t"
  "v_mfma_f32_16x16x32_bf16 v[104:107], v[244:247], v[196:199], v[104:107]\n\t"
  "ds_read_b64_tr_b16 v[234:235], v252 offset:41472\n\t"
  "v_mfma_f32_16x16x32_bf16 v[128:131], v[244:247], v[204:207], v[128:131]\n\t"
  "ds_read_b64_tr_b16 v[236:237], v252 offset:45056\n\t"
  "v_mfma_f32_16x16x32_bf16 v[128:131], v[240:243], v[200:203], v[128:131]\n\t"
  "ds_read_b64_tr_b16 v[238:239], v252 offset:45568\n\t"
  "v_mfma_f32_16x16x32_bf16 v[152:155], v[240:243], v[208:211], v[152:155]\n\t"
  "v_mfma_f32_16x16x32_bf16 v[152:155], v[244:247], v[212:215], v[152:155]\n\t"
  "v_mfma_f32_16x16x32_bf16 v[176:179], v[244:247], v[220:223], v[176:179]\n\t"
  "v_mfma_f32_16x16x32_bf16 v[176:179], v[240:243], v[216:219], v[176:179]\n\t"
  "s_waitcnt lgkmcnt(6)\n\t"
  "v_mfma_f32_16x16x32_bf16 v[180:183], v[224:227], v[216:219], v[180:183]\n\t"
  "ds_read_b64_tr_b16 v[240:241], v253 offset:40960\n\t"
  "s_waitcnt lgkmcnt(5)\n\t"
  "v_mfma_f32_16x16x32_bf16 v[180:183], v[228:231], v[220:223], v[180:183]\n\t"
  "ds_read_b64_tr_b16 v[242:243], v253 offset:41472\n\t"
  "v_mfma_f32_16x16x32_bf16 v[156:159], v[228:231], v[212:215], v[156:159]\n\t"
  "ds_read_b64_tr_b16 v[244:245], v253 offset:45056\n\t"
  "v_mfma_f32_16x16x32_bf16 v[156:159], v[224:227], v[208:211], v[156:159]\n\t"
  "ds_read_b64_tr_b16 v[246:247], v253 offset:45568\n\t"
  "v_mfma_f32_16x16x32_bf16 v[132:135], v[224:227], v[200:203], v[132:135]\n\t"
  "v_mfma_f32_16x16x32_bf16 v[132:135], v[228:231], v[204:207], v[132:135]\n\t"
  "v_mfma_f32_16x16x32_bf16 v[108:111], v[228:231], v[196:199], v[108:111]\n\t"
  "v_mfma_f32_16x16x32_bf16 v[108:111], v[224:227], v[192:195], v[108:111]\n\t"
  "s_waitcnt lgkmcnt(4)\n\t"
  "v_mfma_f32_16x16x32_bf16 v[112:115], v[192:195], v[232:235], v[112:115]\n\t"
  "v_mfma_f32_16x16x32_bf16 v[112:115], v[196:199], v[236:239], v[112:115]\n\t"
  "s_sub_u32 s40, s40, 1\n\t"
  "v_mfma_f32_16x16x32_bf16 v[136:139], v[204:207], v[236:239], v[136:139]\n\t"
  "s_cmp_eq_u32 s40, 0\n\t"
  "v_mfma_f32_16x16x32_bf16 v[136:139], v[200:203], v[232:235], v[136:139]\n\t"
  "s_waitcnt lgkmcnt(0)\n\t"
  "s_barrier\n\t"
  "v_mfma_f32_16x16x32_bf16 v[160:163], v[208:211], v[232:235], v[160:163]\n\t"
  "ds_read_b64_tr_b16 v[224:225], v250 offset:0\n\t"
  "v_mfma_f32_16x16x32_bf16 v[160:163], v[212:215], v[236:239], v[160:163]\n\t"
  "ds_read_b64_tr_b16 v[226:227], v250 offset:512\n\t"
  "v_mfma_f32_16x16x32_bf16 v[184:187], v[220:223], v[236:239], v[184:187]\n\t"
  "ds_read_b64_tr_b16 v[228:229], v250 offset:4096\n\t"
  "v_mfma_f32_16x16x32_bf16 v[184:187], v[216:219], v[232:235], v[184:187]\n\t"
  "ds_read_b64_tr_b16 v[230:231], v250 offset:4608\n\t"
  "v_mfma_f32_16x16x32_bf16 v[116:119], v[192:195], v[240:243], v[116:119]\n\t"
  "ds_read_b64_tr_b16 v[232:233], v251 offset:0\n\t"
  "v_mfma_f32_16x16x32_bf16 v[116:119], v[196:199], v[244:247], v[116:119]\n\t"
  "ds_read_b128 v[192:195], v248 offset:8192\n\t"
  "ds_read_b128 v[196:199], v249 offset:8192\n\t"
  "v_mfma_f32_16x16x32_bf16 v[140:143], v[204:207], v[244:247], v[140:143]\n\t"
  "ds_read_b64_tr_b16 v[234:235], v251 offset:512\n\t"
  "v_mfma_f32_16x16x32_bf16 v[140:143], v[200:203], v[240:243], v[140:143]\n\t"
  "ds_read_b128 v[200:203], v248 offset:10240\n\t"
  "ds_read_b128 v[204:207], v249 offset:10240\n\t"
  "v_mfma_f32_16x16x32_bf16 v[164:167], v[208:211], v[240:243], v[164:167]\n\t"
  "ds_read_b64_tr_b16 v[236:237], v251 offset:4096\n\t"
  "v_mfma_f32_16x16x32_bf16 v[164:167], v[212:215], v[244:247], v[164:167]\n\t"
  "ds_read_b128 v[208:211], v248 offset:12288\n\t"
  "ds_read_b128 v[212:215], v249 offset:12288\n\t"
  "v_mfma_f32_16x16x32_bf16 v[188:191], v[220:223], v[244:247], v[188:191]\n\t"
  "ds_read_b64_tr_b16 v[238:239], v251 offset:4608\n\t"
  "v_mfma_f32_16x16x32_bf16 v[188:191], v[216:219], v[240:243], v[188:191]\n\t"
  "ds_read_b128 v[216:219], v248 offset:14336\n\t"
  "ds_read_b128 v[220:223], v249 offset:14336\n\t"
  "s_cbranch_scc1 LE%=\n\t"
  "s_waitcnt lgkmcnt(10)\n\t"
  "v_mfma_f32_16x16x32_bf16 v[0:3], v[224:227], v[192:195], v[0:3]\n\t"
  "ds_read_b64_tr_b16 v[240:241], v250 offset:8192\n\t"
  "s_waitcnt lgkmcnt(10)\n\t"
  "v_mfma_f32_16x16x32_bf16 v[0:3], v[228:231], v[196:199], v[0:3]\n\t"
  "ds_read_b64_tr_b16 v[242:243], v250 offset:8704\n\t"
  "s_waitcnt lgkmcnt(8)\n\t"
  "v_mfma_f32_16x16x32_bf16 v[24:27], v[228:231], v[204:207], v[24:27]\n\t"
  "ds_read_b64_tr_b16 v[244:245], v250 offset:12288\n\t"
  "v_mfma_f32_16x16x32_bf16 v[24:27], v[224:227], v[200:203], v[24:27]\n\t"
  "ds_read_b64_tr_b16 v[246:247], v250 offset:12800\n\t"
  "s_waitcnt lgkmcnt(4)\n\t"
  "v_mfma_f32_16x16x32_bf16 v[48:51], v[224:227], v[208:211], v[48:51]\n\t"
  "v_mfma_f32_16x16x32_bf16 v[48:51], v[228:231], v[212:215], v[48:51]\n\t"
  "v_mfma_f32_16x16x32_bf16 v[72:75], v[228:231], v[220:223], v[72:75]\n\t"
  "v_mfma_f32_16x16x32_bf16 v[72:75], v[224:227], v[216:219], v[72:75]\n\t"
  "v_mfma_f32_16x16x32_bf16 v[76:79], v[232:235], v[216:219], v[76:79]\n\t"
  "ds_read_b64_tr_b16 v[224:225], v251 offset:8192\n\t"
  "v_mfma_f32_16x16x32_bf16 v[76:79], v[236:239], v[220:223], v[76:79]\n\t"
  "ds_read_b64_tr_b16 v[226:227], v251 offset:8704\n\t"
  "v_mfma_f32_16x16x32_bf16 v[52:55], v[236:239], v[212:215], v[52:55]\n\t"
  "ds_read_b64_tr_b16 v[228:229], v251 offset:12288\n\t"
  "v_mfma_f32_16x16x32_bf16 v[52:55], v[232:235], v[208:211], v[52:55]\n\t"
  "ds_read_b64_tr_b16 v[230:231], v251 offset:12800\n\t"
  "v_mfma_f32_16x16x32_bf16 v[28:31], v[232:235], v[200:203], v[28:31]\n\t"
  "v_mfma_f32_16x16x32_bf16 v[28:31], v[236:239], v[204:207], v[28:31]\n\t"
  "v_mfma_f32_16x16x32_bf16 v[4:7], v[236:239], v[196:199], v[4:7]\n\t"
  "v_mfma_f32_16x16x32_bf16 v[4:7], v[232:235], v[192:195], v[4:7]\n\t"
  "s_waitcnt lgkmcnt(6)\n\t"
  "v_mfma_f32_16x16x32_bf16 v[8:11], v[240:243], v[192:195], v[8:11]\n\t"
  "ds_read_b64_tr_b16 v[232:233], v250 offset:16384\n\t"
  "s_waitcnt lgkmcnt(5)\n\t"
  "v_mfma_f32_16x16x32_bf16 v[8:11], v[244:247], v[196:199], v[8:11]\n\t"
  "ds_read_b64_tr_b16 v[234:235], v250 offset:16896\n\t"
  "v_mfma_f32_16x16x32_bf16 v[32:35], v[244:247], v[204:207], v[32:35]\n\t"
  "ds_read_b64_tr_b16 v[236:237], v250 offset:20480\n\t"
  "v_mfma_f32_16x16x32_bf16 v[32:35], v[240:243], v[200:203], v[32:35]\n\t"
  "ds_read_b64_tr_b16 v[238:239], v250 offset:20992\n\t"
  "v_mfma_f32_16x16x32_bf16 v[56:59], v[240:243], v[208:211], v[56:59]\n\t"
  "v_mfma_f32_16x16x32_bf16 v[56:59], v[244:247], v[212:215], v[56:59]\n\t"
  "v_mfma_f32_16x16x32_bf16 v[80:83], v[244:247], v[220:223], v[80:83]\n\t"
  "v_mfma_f32_16x16x32_bf16 v[80:83], v[240:243], v[216:219], v[80:83]\n\t"
  "s_waitcnt lgkmcnt(6)\n\t"
  "v_mfma_f32_16x16x32_bf16 v[84:87], v[224:227], v[216:219], v[84:87]\n\t"
  "ds_read_b64_tr_b16 v[240:241], v251 offset:16384\n\t"
  "s_waitcnt lgkmcnt(5)\n\t"
  "v_mfma_f32_16x16x32_bf16 v[84:87], v[228:231], v[220:223], v[84:87]\n\t"
  "ds_read_b64_tr_b16 v[242:243], v251 offset:16896\n\t"
  "v_mfma_f32_16x16x32_bf16 v[60:63], v[228:231], v[212:215], v[60:63]\n\t"
  "ds_read_b64_tr_b16 v[244:245], v251 offset:20480\n\t"
  "v_mfma_f32_16x16x32_bf16 v[60:63], v[224:227], v[208:211], v[60:63]\n\t"
  "ds_read_b64_tr_b16 v[246:247], v251 offset:20992\n\t"
  "v_mfma_f32_16x16x32_bf16 v[36:39], v[224:227], v[200:203], v[36:39]\n\t"
  "v_mfma_f32_16x16x32_bf16 v[36:39], v[228:231], v[204:207], v[36:39]\n\t"
  "v_mfma_f32_16x16x32_bf16 v[12:15], v[228:231], v[196:199], v[12:15]\n\t"
  "v_mfma_f32_16x16x32_bf16 v[12:15], v[224:227], v[192:195], v[12:15]\n\t"
  "s_waitcnt lgkmcnt(6)\n\t"
  "v_mfma_f32_16x16x32_bf16 v[16:19], v[192:195], v[232:235], v[16:19]\n\t"
  "ds_read_b64_tr_b16 v[224:225], v252 offset:0\n\t"
  "s_waitcnt lgkmcnt(5)\n\t"
  "v_mfma_f32_16x16x32_bf16 v[16:19], v[196:199], v[236:239], v[16:19]\n\t"
  "ds_read_b64_tr_b16 v[226:227], v252 offset:512\n\t"
  "v_mfma_f32_16x16x32_bf16 v[40:43], v[204:207], v[236:239], v[40:43]\n\t"
  "ds_read_b64_tr_b16 v[228:229], v252 offset:4096\n\t"
  "v_mfma_f32_16x16x32_bf16 v[40:43], v[200:203], v[232:235], v[40:43]\n\t"
  "ds_read_b64_tr_b16 v[230:231], v252 offset:4608\n\t"
  "v_mfma_f32_16x16x32_bf16 v[64:67], v[208:211], v[232:235], v[64:67]\n\t"
  "v_mfma_f32_16x16x32_bf16 v[64:67], v[212:215], v[236:239], v[64:67]\n\t"
  "v_mfma_f32_16x16x32_bf16 v[88:91], v[220:223], v[236:239], v[88:91]\n\t"
  "v_mfma_f32_16x16x32_bf16 v[88:91], v[216:219], v[232:235], v[88:91]\n\t"
  "s_waitcnt lgkmcnt(6)\n\t"
  "v_mfma_f32_16x16x32_bf16 v[92:95], v[216:219], v[240:243], v[92:95]\n\t"
  "ds_read_b64_tr_b16 v[232:233], v253 offset:0\n\t"
  "s_waitcnt lgkmcnt(5)\n\t"
  "v_mfma_f32_16x16x32_bf16 v[92:95], v[220:223], v[244:247], v[92:95]\n\t"
  "ds_read_b64_tr_b16 v[234:235], v253 offset:512\n\t"
  "v_mfma_f32_16x16x32_bf16 v[68:71], v[212:215], v[244:247], v[68:71]\n\t"
  "ds_read_b64_tr_b16 v[236:237], v253 offset:4096\n\t"
  "v_mfma_f32_16x16x32_bf16 v[68:71], v[208:211], v[240:243], v[68:71]\n\t"
  "ds_read_b64_tr_b16 v[238:239], v253 offset:4608\n\t"
  "v_mfma_f32_16x16x32_bf16 v[44:47], v[200:203], v[240:243], v[44:47]\n\t"
  "v_mfma_f32_16x16x32_bf16 v[44:47], v[204:207], v[244:247], v[44:47]\n\t"
  "v_mfma_f32_16x16x32_bf16 v[20:23], v[196:199], v[244:247], v[20:23]\n\t"
  "v_mfma_f32_16x16x32_bf16 v[20:23], v[192:195], v[240:243], v[20:23]\n\t"
  "s_waitcnt lgkmcnt(6)\n\t"
  "v_mfma_f32_16x16x32_bf16 v[96:99], v[224:227], v[192:195], v[96:99]\n\t"
  "ds_read_b64_tr_b16 v[240:241], v252 offset:8192\n\t"
  "s_waitcnt lgkmcnt(5)\n\t"
  "v_mfma_f32_16x16x32_bf16 v[96:99], v[228:231], v[196:199], v[96:99]\n\t"
  "ds_read_b64_tr_b16 v[242:243], v252 offset:8704\n\t"
  "v_mfma_f32_16x16x32_bf16 v[120:123], v[228:231], v[204:207], v[120:123]\n\t"
  "ds_read_b64_tr_b16 v[244:245], v252 offset:12288\n\t"
  "v_mfma_f32_16x16x32_bf16 v[120:123], v[224:227], v[200:203], v[120:123]\n\t"
  "ds_read_b64_tr_b16 v[246:247], v252 offset:12800\n\t"
  "v_mfma_f32_16x16x32_bf16 v[144:147], v[224:227], v[208:211], v[144:147]\n\t"
  "v_mfma_f32_16x16x32_bf16 v[144:147], v[228:231], v[212:215], v[144:147]\n\t"
  "v_mfma_f32_16x16x32_bf16 v[168:171], v[228:231], v[220:223], v[168:171]\n\t"
  "v_mfma_f32_16x16x32_bf16 v[168:171], v[224:227], v[216:219], v[168:171]\n\t"
  "s_waitcnt lgkmcnt(6)\n\t"
  "v_mfma_f32_16x16x32_bf16 v[172:175], v[232:235], v[216:219], v[172:175]\n\t"
  "ds_read_b64_tr_b16 v[224:225], v253 offset:8192\n\t"
  "s_waitcnt lgkmcnt(5)\n\t"
  "v_mfma_f32_16x16x32_bf16 v[172:175], v[236:239], v[220:223], v[172:175]\n\t"
  "ds_read_b64_tr_b16 v[226:227], v253 offset:8704\n\t"
  "v_mfma_f32_16x16x32_bf16 v[148:151], v[236:239], v[212:215], v[148:151]\n\t"
  "ds_read_b64_tr_b16 v[228:229], v253 offset:12288\n\t"
  "v_mfma_f32_16x16x32_bf16 v[148:151], v[232:235], v[208:211], v[148:151]\n\t"
  "ds_read_b64_tr_b16 v[230:231], v253 offset:12800\n\t"
  "v_mfma_f32_16x16x32_bf16 v[124:127], v[232:235], v[200:203], v[124:127]\n\t"
  "v_mfma_f32_16x16x32_bf16 v[124:127], v[236:239], v[204:207], v[124:127]\n\t"
  "v_mfma_f32_16x16x32_bf16 v[100:103], v[236:239], v[196:199], v[100:103]\n\t"
  "v_mfma_f32_16x16x32_bf16 v[100:103], v[232:235], v[192:195], v[100:103]\n\t"
  "s_waitcnt lgkmcnt(6)\n\t"
  "v_mfma_f32_16x16x32_bf16 v[104:107], v[240:243], v[192:195], v[104:107]\n\t"
  "ds_read_b64_tr_b16 v[232:233], v252 offset:16384\n\t"
  "s_waitcnt lgkmcnt(5)\n\t"
  "v_mfma_f32_16x16x32_bf16 v[104:107], v[244:247], v[196:199], v[104:107]\n\t"
  "ds_read_b64_tr_b16 v[234:235], v252 offset:16896\n\t"
  "v_mfma_f32_16x16x32_bf16 v[128:131], v[244:247], v[204:207], v[128:131]\n\t"
  "ds_read_b64_tr_b16 v[236:237], v252 offset:20480\n\t"
  "v_mfma_f32_16x16x32_bf16 v[128:131], v[240:243], v[200:203], v[128:131]\n\t"
  "ds_read_b64_tr_b16 v[238:239], v252 offset:20992\n\t"
  "v_mfma_f32_16x16x32_bf16 v[152:155], v[240:243], v[208:211], v[152:155]\n\t"
  "v_mfma_f32_16x16x32_bf16 v[152:155], v[244:247], v[212:215], v[152:155]\n\t"
  "v_mfma_f32_16x16x32_bf16 v[176:179], v[244:247], v[220:223], v[176:179]\n\t"
  "v_mfma_f32_16x16x32_bf16 v[176:179], v[240:243], v[216:219], v[176:179]\n\t"
  "s_waitcnt lgkmcnt(6)\n\t"
  "v_mfma_f32_16x16x32_bf16 v[180:183], v[224:227], v[216:219], v[180:183]\n\t"
  "ds_read_b64_tr_b16 v[240:241], v253 offset:16384\n\t"
  "s_waitcnt lgkmcnt(5)\n\t"
  "v_mfma_f32_16x16x32_bf16 v[180:183], v[228:231], v[220:223], v[180:183]\n\t"
  "ds_read_b64_tr_b16 v[242:243], v253 offset:16896\n\t"
  "v_mfma_f32_16x16x32_bf16 v[156:159], v[228:231], v[212:215], v[156:159]\n\t"
  "ds_read_b64_tr_b16 v[244:245], v253 offset:20480\n\t"
  "v_mfma_f32_16x16x32_bf16 v[156:159], v[224:227], v[208:211], v[156:159]\n\t"
  "ds_read_b64_tr_b16 v[246:247], v253 offset:20992\n\t"
  "v_mfma_f32_16x16x32_bf16 v[132:135], v[224:227], v[200:203], v[132:135]\n\t"
  "v_mfma_f32_16x16x32_bf16 v[132:135], v[228:231], v[204:207], v[132:135]\n\t"
  "v_mfma_f32_16x16x32_bf16 v[108:111], v[228:231], v[196:199], v[108:111]\n\t"
  "v_mfma_f32_16x16x32_bf16 v[108:111], v[224:227], v[192:195], v[108:111]\n\t"
  "s_waitcnt lgkmcnt(4)\n\t"
  "v_mfma_f32_16x16x32_bf16 v[112:115], v[192:195], v[232:235], v[112:115]\n\t"
  "v_mfma_f32_16x16x32_bf16 v[112:115], v[196:199], v[236:239], v[112:115]\n\t"
  "v_mfma_f32_16x16x32_bf16 v[136:139], v[204:207], v[236:239], v[136:139]\n\t"
  "v_mfma_f32_16x16x32_bf16 v[136:139], v[200:203], v[232:235], v[136:139]\n\t"
  "s_waitcnt lgkmcnt(0)\n\t"
  "s_barrier\n\t"
  "v_mfma_f32_16x16x32_bf16 v[160:163], v[208:211], v[232:235], v[160:163]\n\t"
  "ds_read_b64_tr_b16 v[224:225], v250 offset:24576\n\t"
  "v_mfma_f32_16x16x32_bf16 v[160:163], v[212:215], v[236:239], v[160:163]\n\t"
  "ds_read_b64_tr_b16 v[226:227], v250 offset:25088\n\t"
  "v_mfma_f32_16x16x32_bf16 v[184:187], v[220:223], v[236:239], v[184:187]\n\t"
  "ds_read_b64_tr_b16 v[228:229], v250 offset:28672\n\t"
  "v_mfma_f32_16x16x32_bf16 v[184:187], v[216:219], v[232:235], v[184:187]\n\t"
  "ds_read_b64_tr_b16 v[230:231], v250 offset:29184\n\t"
  "v_mfma_f32_16x16x32_bf16 v[116:119], v[192:195], v[240:243], v[116:119]\n\t"
  "ds_read_b64_tr_b16 v[232:233], v251 offset:24576\n\t"
  "v_mfma_f32_16x16x32_bf16 v[116:119], v[196:199], v[244:247], v[116:119]\n\t"
  "ds_read_b128 v[192:195], v248 offset:16384\n\t"
  "ds_read_b128 v[196:199], v249 offset:16384\n\t"
  "v_mfma_f32_16x16x32_bf16 v[140:143], v[204:207], v[244:247], v[140:143]\n\t"
  "ds_read_b64_tr_b16 v[234:235], v251 offset:25088\n\t"
  "v_mfma_f32_16x16x32_bf16 v[140:143], v[200:203], v[240:243], v[140:143]\n\t"
  "ds_read_b128 v[200:203], v248 offset:18432\n\t"
  "ds_read_b128 v[204:207], v249 offset:18432\n\t"
  "v_mfma_f32_16x16x32_bf16 v[164:167], v[208:211], v[240:243], v[164:167]\n\t"
  "ds_read_b64_tr_b16 v[236:237], v251 offset:28672\n\t"
  "v_mfma_f32_16x16x32_bf16 v[164:167], v[212:215], v[244:247], v[164:167]\n\t"
  "ds_read_b128 v[208:211], v248 offset:20480\n\t"
  "ds_read_b128 v[212:215], v249 offset:20480\n\t"
  "v_mfma_f32_16x16x32_bf16 v[188:191], v[220:223], v[244:247], v[188:191]\n\t"
  "ds_read_b64_tr_b16 v[238:239], v251 offset:29184\n\t"
  "v_mfma_f32_16x16x32_bf16 v[188:191], v[216:219], v[240:243], v[188:191]\n\t"
  "ds_read_b128 v[216:219], v248 offset:22528\n\t"
  "ds_read_b128 v[220:223], v249 offset:22528\n\t"
  "s_waitcnt lgkmcnt(10)\n\t"
  "v_mfma_f32_16x16x32_bf16 v[0:3], v[224:227], v[192:195], v[0:3]\n\t"
  "ds_read_b64_tr_b16 v[240:241], v250 offset:32768\n\t"
  "s_waitcnt lgkmcnt(10)\n\t"
  "v_mfma_f32_16x16x32_bf16 v[0:3], v[228:231], v[196:199], v[0:3]\n\t"
  "ds_read_b64_tr_b16 v[242:243], v250 offset:33280\n\t"
  "s_waitcnt lgkmcnt(8)\n\t"
  "v_mfma_f32_16x16x32_bf16 v[24:27], v[228:231], v[204:207], v[24:27]\n\t"
  "ds_read_b64_tr_b16 v[244:245], v250 offset:36864\n\t"
  "v_mfma_f32_16x16x32_bf16 v[24:27], v[224:227], v[200:203], v[24:27]\n\t"
  "ds_read_b64_tr_b16 v[246:247], v250 offset:37376\n\t"
  "s_waitcnt lgkmcnt(4)\n\t"
  "v_mfma_f32_16x16x32_bf16 v[48:51], v[224:227], v[208:211], v[48:51]\n\t"
  "v_mfma_f32_16x16x32_bf16 v[48:51], v[228:231], v[212:215], v[48:51]\n\t"
  "v_mfma_f32_16x16x32_bf16 v[72:75], v[228:231], v[220:223], v[72:75]\n\t"
  "v_mfma_f32_16x16x32_bf16 v[72:75], v[224:227], v[216:219], v[72:75]\n\t"
  "v_mfma_f32_16x16x32_bf16 v[76:79], v[232:235], v[216:219], v[76:79]\n\t"
  "ds_read_b64_tr_b16 v[224:225], v251 offset:32768\n\t"
  "v_mfma_f32_16x16x32_bf16 v[76:79], v[236:239], v[220:223], v[76:79]\n\t"
  "ds_read_b64_tr_b16 v[226:227], v251 offset:33280\n\t"
  "v_mfma_f32_16x16x32_bf16 v[52:55], v[236:239], v[212:215], v[52:55]\n\t"
  "ds_read_b64_tr_b16 v[228:229], v251 offset:36864\n\t"
  "v_mfma_f32_16x16x32_bf16 v[52:55], v[232:235], v[208:211], v[52:55]\n\t"
  "ds_read_b64_tr_b16 v[230:231], v251 offset:37376\n\t"
  "v_mfma_f32_16x16x32_bf16 v[28:31], v[232:235], v[200:203], v[28:31]\n\t"
  "v_mfma_f32_16x16x32_bf16 v[28:31], v[236:239], v[204:207], v[28:31]\n\t"
  "v_mfma_f32_16x16x32_bf16 v[4:7], v[236:239], v[196:199], v[4:7]\n\t"
  "v_mfma_f32_16x16x32_bf16 v[4:7], v[232:235], v[192:195], v[4:7]\n\t"
  "s_waitcnt lgkmcnt(6)\n\t"
  "v_mfma_f32_16x16x32_bf16 v[8:11], v[240:243], v[192:195], v[8:11]\n\t"
  "ds_read_b64_tr_b16 v[232:233], v250 offset:40960\n\t"
  "s_waitcnt lgkmcnt(5)\n\t"
  "v_mfma_f32_16x16x32_bf16 v[8:11], v[244:247], v[196:199], v[8:11]\n\t"
  "ds_read_b64_tr_b16 v[234:235], v250 offset:41472\n\t"
  "v_mfma_f32_16x16x32_bf16 v[32:35], v[244:247], v[204:207], v[32:35]\n\t"
  "ds_read_b64_tr_b16 v[236:237], v250 offset:45056\n\t"
  "v_mfma_f32_16x16x32_bf16 v[32:35], v[240:243], v[200:203], v[32:35]\n\t"
  "ds_read_b64_tr_b16 v[238:239], v250 offset:45568\n\t"
  "v_mfma_f32_16x16x32_bf16 v[56:59], v[240:243], v[208:211], v[56:59]\n\t"
  "v_mfma_f32_16x16x32_bf16 v[56:59], v[244:247], v[212:215], v[56:59]\n\t"
  "v_mfma_f32_16x16x32_bf16 v[80:83], v[244:247], v[220:223], v[80:83]\n\t"
  "v_mfma_f32_16x16x32_bf16 v[80:83], v[240:243], v[216:219], v[80:83]\n\t"
  "s_waitcnt lgkmcnt(6)\n\t"
  "v_mfma_f32_16x16x32_bf16 v[84:87], v[224:227], v[216:219], v[84:87]\n\t"
  "ds_read_b64_tr_b16 v[240:241], v251 offset:40960\n\t"
  "s_waitcnt lgkmcnt(5)\n\t"
  "v_mfma_f32_16x16x32_bf16 v[84:87], v[228:231], v[220:223], v[84:87]\n\t"
  "ds_read_b64_tr_b16 v[242:243], v251 offset:41472\n\t"
  "v_mfma_f32_16x16x32_bf16 v[60:63], v[228:231], v[212:215], v[60:63]\n\t"
  "ds_read_b64_tr_b16 v[244:245], v251 offset:45056\n\t"
  "v_mfma_f32_16x16x32_bf16 v[60:63], v[224:227], v[208:211], v[60:63]\n\t"
  "ds_read_b64_tr_b16 v[246:247], v251 offset:45568\n\t"
  "v_mfma_f32_16x16x32_bf16 v[36:39], v[224:227], v[200:203], v[36:39]\n\t"
  "v_mfma_f32_16x16x32_bf16 v[36:39], v[228:231], v[204:207], v[36:39]\n\t"
  "v_mfma_f32_16x16x32_bf16 v[12:15], v[228:231], v[196:199], v[12:15]\n\t"
  "v_mfma_f32_16x16x32_bf16 v[12:15], v[224:227], v[192:195], v[12:15]\n\t"
  "s_waitcnt lgkmcnt(6)\n\t"
  "v_mfma_f32_16x16x32_bf16 v[16:19], v[192:195], v[232:235], v[16:19]\n\t"
  "ds_read_b64_tr_b16 v[224:225], v252 offset:24576\n\t"
  "s_waitcnt lgkmcnt(5)\n\t"
  "v_mfma_f32_16x16x32_bf16 v[16:19], v[196:199], v[236:239], v[16:19]\n\t"
  "ds_read_b64_tr_b16 v[226:227], v252 offset:25088\n\t"
  "v_mfma_f32_16x16x32_bf16 v[40:43], v[204:207], v[236:239], v[40:43]\n\t"
  "ds_read_b64_tr_b16 v[228:229], v252 offset:28672\n\t"
  "v_mfma_f32_16x16x32_bf16 v[40:43], v[200:203], v[232:235], v[40:43]\n\t"
  "ds_read_b64_tr_b16 v[230:231], v252 offset:29184\n\t"
  "v_mfma_f32_16x16x32_bf16 v[64:67], v[208:211], v[232:235], v[64:67]\n\t"
  "v_mfma_f32_16x16x32_bf16 v[64:67], v[212:215], v[236:239], v[64:67]\n\t"
  "v_mfma_f32_16x16x32_bf16 v[88:91], v[220:223], v[236:239], v[88:91]\n\t"
  "v_mfma_f32_16x16x32_bf16 v[88:91], v[216:219], v[232:235], v[88:91]\n\t"
  "s_waitcnt lgkmcnt(6)\n\t"
  "v_mfma_f32_16x16x32_bf16 v[92:95], v[216:219], v[240:243], v[92:95]\n\t"
  "ds_read_b64_tr_b16 v[232:233], v253 offset:24576\n\t"
  "s_waitcnt lgkmcnt(5)\n\t"
  "v_mfma_f32_16x16x32_bf16 v[92:95], v[220:223], v[244:247], v[92:95]\n\t"
  "ds_read_b64_tr_b16 v[234:235], v253 offset:25088\n\t"
  "v_mfma_f32_16x16x32_bf16 v[68:71], v[212:215], v[244:247], v[68:71]\n\t"
  "ds_read_b64_tr_b16 v[236:237], v253 offset:28672\n\t"
  "v_mfma_f32_16x16x32_bf16 v[68:71], v[208:211], v[240:243], v[68:71]\n\t"
  "ds_read_b64_tr_b16 v[238:239], v253 offset:29184\n\t"
  "v_mfma_f32_16x16x32_bf16 v[44:47], v[200:203], v[240:243], v[44:47]\n\t"
  "v_mfma_f32_16x16x32_bf16 v[44:47], v[204:207], v[244:247], v[44:47]\n\t"
  "v_mfma_f32_16x16x32_bf16 v[20:23], v[196:199], v[244:247], v[20:23]\n\t"
  "v_mfma_f32_16x16x32_bf16 v[20:23], v[192:195], v[240:243], v[20:23]\n\t"
  "s_waitcnt lgkmcnt(6)\n\t"
  "v_mfma_f32_16x16x32_bf16 v[96:99], v[224:227], v[192:195], v[96:99]\n\t"
  "ds_read_b64_tr_b16 v[240:241], v252 offset:32768\n\t"
  "s_waitcnt lgkmcnt(5)\n\t"
  "v_mfma_f32_16x16x32_bf16 v[96:99], v[228:231], v[196:199], v[96:99]\n\t"
  "ds_read_b64_tr_b16 v[242:243], v252 offset:33280\n\t"
  "v_mfma_f32_16x16x32_bf16 v[120:123], v[228:231], v[204:207], v[120:123]\n\t"
  "ds_read_b64_tr_b16 v[244:245], v252 offset:36864\n\t"
  "v_mfma_f32_16x16x32_bf16 v[120:123], v[224:227], v[200:203], v[120:123]\n\t"
  "ds_read_b64_tr_b16 v[246:247], v252 offset:37376\n\t"
  "v_mfma_f32_16x16x32_bf16 v[144:147], v[224:227], v[208:211], v[144:147]\n\t"
  "v_mfma_f32_16x16x32_bf16 v[144:147], v[228:231], v[212:215], v[144:147]\n\t"
  "v_mfma_f32_16x16x32_bf16 v[168:171], v[228:231], v[220:223], v[168:171]\n\t"
  "v_mfma_f32_16x16x32_bf16 v[168:171], v[224:227], v[216:219], v[168:171]\n\t"
  "s_waitcnt lgkmcnt(6)\n\t"
  "v_mfma_f32_16x16x32_bf16 v[172:175], v[232:235], v[216:219], v[172:175]\n\t"
  "ds_read_b64_tr_b16 v[224:225], v253 offset:32768\n\t"
  "s_waitcnt lgkmcnt(5)\n\t"
  "v_mfma_f32_16x16x32_bf16 v[172:175], v[236:239], v[220:223], v[172:175]\n\t"
  "ds_read_b64_tr_b16 v[226:227], v253 offset:33280\n\t"
  "v_mfma_f32_16x16x32_bf16 v[148:151], v[236:239], v[212:215], v[148:151]\n\t"
  "ds_read_b64_tr_b16 v[228:229], v253 offset:36864\n\t"
  "v_mfma_f32_16x16x32_bf16 v[148:151], v[232:235], v[208:211], v[148:151]\n\t"
  "ds_read_b64_tr_b16 v[230:231], v253 offset:37376\n\t"
  "v_mfma_f32_16x16x32_bf16 v[124:127], v[232:235], v[200:203], v[124:127]\n\t"
  "v_mfma_f32_16x16x32_bf16 v[124:127], v[236:239], v[204:207], v[124:127]\n\t"
  "v_mfma_f32_16x16x32_bf16 v[100:103], v[236:239], v[196:199], v[100:103]\n\t"
  "v_mfma_f32_16x16x32_bf16 v[100:103], v[232:235], v[192:195], v[100:103]\n\t"
  "s_waitcnt lgkmcnt(6)\n\t"
  "v_mfma_f32_16x16x32_bf16 v[104:107], v[240:243], v[192:195], v[104:107]\n\t"
  "ds_read_b64_tr_b16 v[232:233], v252 offset:40960\n\t"
  "s_waitcnt lgkmcnt(5)\n\t"
  "v_mfma_f32_16x16x32_bf16 v[104:107], v[244:247], v[196:199], v[104:107]\n\t"
  "ds_read_b64_tr_b16 v[234:235], v252 offset:41472\n\t"
  "v_mfma_f32_16x16x32_bf16 v[128:131], v[244:247], v[204:207], v[128:131]\n\t"
  "ds_read_b64_tr_b16 v[236:237], v252 offset:45056\n\t"
  "v_mfma_f32_16x16x32_bf16 v[128:131], v[240:243], v[200:203], v[128:131]\n\t"
  "ds_read_b64_tr_b16 v[238:239], v252 offset:45568\n\t"
  "v_mfma_f32_16x16x32_bf16 v[152:155], v[240:243], v[208:211], v[152:155]\n\t"
  "v_mfma_f32_16x16x32_bf16 v[152:155], v[244:247], v[212:215], v[152:155]\n\t"
  "v_mfma_f32_16x16x32_bf16 v[176:179], v[244:247], v[220:223], v[176:179]\n\t"
  "v_mfma_f32_16x16x32_bf16 v[176:179], v[240:243], v[216:219], v[176:179]\n\t"
  "s_waitcnt lgkmcnt(6)\n\t"
  "v_mfma_f32_16x16x32_bf16 v[180:183], v[224:227], v[216:219], v[180:183]\n\t"
  "ds_read_b64_tr_b16 v[240:241], v253 offset:40960\n\t"
  "s_waitcnt lgkmcnt(5)\n\t"
  "v_mfma_f32_16x16x32_bf16 v[180:183], v[228:231], v[220:223], v[180:183]\n\t"
  "ds_read_b64_tr_b16 v[242:243], v253 offset:41472\n\t"
  "v_mfma_f32_16x16x32_bf16 v[156:159], v[228:231], v[212:215], v[156:159]\n\t"
  "ds_read_b64_tr_b16 v[244:245], v253 offset:45056\n\t"
  "v_mfma_f32_16x16x32_bf16 v[156:159], v[224:227], v[208:211], v[156:159]\n\t"
  "ds_read_b64_tr_b16 v[246:247], v253 offset:45568\n\t"
  "v_mfma_f32_16x16x32_bf16 v[132:135], v[224:227], v[200:203], v[132:135]\n\t"
  "v_mfma_f32_16x16x32_bf16 v[132:135], v[228:231], v[204:207], v[132:135]\n\t"
  "v_mfma_f32_16x16x32_bf16 v[108:111], v[228:231], v[196:199], v[108:111]\n\t"
  "v_mfma_f32_16x16x32_bf16 v[108:111], v[224:227], v[192:195], v[108:111]\n\t"
  "s_waitcnt lgkmcnt(4)\n\t"
  "v_mfma_f32_16x16x32_bf16 v[112:115], v[192:195], v[232:235], v[112:115]\n\t"
  "v_mfma_f32_16x16x32_bf16 v[112:115], v[196:199], v[236:239], v[112:115]\n\t"
  "v_mfma_f32_16x16x32_bf16 v[136:139], v[204:207], v[236:239], v[136:139]\n\t"
  "v_mfma_f32_16x16x32_bf16 v[136:139], v[200:203], v[232:235], v[136:139]\n\t"
  "s_waitcnt lgkmcnt(0)\n\t"
  "s_barrier\n\t"
  "v_mfma_f32_16x16x32_bf16 v[160:163], v[208:211], v[232:235], v[160:163]\n\t"
  "ds_read_b64_tr_b16 v[224:225], v250 offset:0\n\t"
  "v_mfma_f32_16x16x32_bf16 v[160:163], v[212:215], v[236:239], v[160:163]\n\t"
  "ds_read_b64_tr_b16 v[226:227], v250 offset:512\n\t"
  "v_mfma_f32_16x16x32_bf16 v[184:187], v[220:223], v[236:239], v[184:187]\n\t"
  "ds_read_b64_tr_b16 v[228:229], v250 offset:4096\n\t"
  "v_mfma_f32_16x16x32_bf16 v[184:187], v[216:219], v[232:235], v[184:187]\n\t"
  "ds_read_b64_tr_b16 v[230:231], v250 offset:4608\n\t"
  "v_mfma_f32_16x16x32_bf16 v[116:119], v[192:195], v[240:243], v[116:119]\n\t"
  "ds_read_b64_tr_b16 v[232:233], v251 offset:0\n\t"
  "v_mfma_f32_16x16x32_bf16 v[116:119], v[196:199], v[244:247], v[116:119]\n\t"
  "ds_read_b128 v[192:195], v248 offset:0\n\t"
  "ds_read_b128 v[196:199], v249 offset:0\n\t"
  "v_mfma_f32_16x16x32_bf16 v[140:143], v[204:207], v[244:247], v[140:143]\n\t"
  "ds_read_b64_tr_b16 v[234:235], v251 offset:512\n\t"
  "v_mfma_f32_16x16x32_bf16 v[140:143], v[200:203], v[240:243], v[140:143]\n\t"
  "ds_read_b128 v[200:203], v248 offset:2048\n\t"
  "ds_read_b128 v[204:207], v249 offset:2048\n\t"
  "v_mfma_f32_16x16x32_bf16 v[164:167], v[208:211], v[240:243], v[164:167]\n\t"
  "ds_read_b64_tr_b16 v[236:237], v251 offset:4096\n\t"
  "v_mfma_f32_16x16x32_bf16 v[164:167], v[212:215], v[244:247], v[164:167]\n\t"
  "ds_read_b128 v[208:211], v248 offset:4096\n\t"
  "ds_read_b128 v[212:215], v249 offset:4096\n\t"
  "v_mfma_f32_16x16x32_bf16 v[188:191], v[220:223], v[244:247], v[188:191]\n\t"
  "ds_read_b64_tr_b16 v[238:239], v251 offset:4608\n\t"
  "v_mfma_f32_16x16x32_bf16 v[188:191], v[216:219], v[240:243], v[188:191]\n\t"
  "ds_read_b128 v[216:219], v248 offset:6144\n\t"
  "ds_read_b128 v[220:223], v249 offset:6144\n\t"
  "s_branch LP%=\n\t"
  "LE%=:\n\t"
  "s_waitcnt lgkmcnt(0)\n\t"
  "s_nop 7\n\t"
  : "={v[0:31]}"(ac[0]), "={v[32:63]}"(ac[1]), "={v[64:95]}"(ac[2]), "={v[96:127]}"(ac[3]), "={v[128:159]}"(ac[4]), "={v[160:191]}"(ac[5]) : "{v248}"(a0), "{v249}"(a0 ^ 64), "{v250}"(b0), "{v251}"(b0 ^ 32), "{v252}"(b0 ^ 64), "{v253}"(b0 ^ 96) : "memory","scc","s40","v192","v193","v194","v195","v196","v197","v198","v199","v200","v201","v202","v203","v204","v205","v206","v207","v208","v209","v210","v211","v212","v213","v214","v215","v216","v217","v218","v219","v220","v221","v222","v223","v224","v225","v226","v227","v228","v229","v230","v231","v232","v233","v234","v235","v236","v237","v238","v239","v240","v241","v242","v243","v244","v245","v246","v247");
      }
      LANES;
      const bool c0 = (mc0 >> lane) & 1, c1 = (mc1 >> lane) & 1;
      const u64 mpar = 0xaaaaaaaaaaaaaaaaull;
      const unsigned q0 = (l15 & 1) ? 0x01000c0cu : 0x0c0c0100u, q1 = q0 + ((l15 & 1) ? 0x02020000u : 0x00000202u);
      const unsigned ps0 = c0 ? 0x0c0c0c0cu : q0, ps1 = c1 ? 0x0c0c0c0cu : q1, ps2 = c0 ? q0 : 0x0c0c0c0cu, ps3 = c1 ? q1 : 0x0c0c0c0cu;
      const unsigned xr = xb + lane * 16, ga = L_G + (wid * 64 + l15 * 4) * 4;
      asm volatile(
  "v_cvt_pk_bf16_f32 v208, v80, v81\n\t"
  "v_cvt_pk_bf16_f32 v209, v82, v83\n\t"
  "v_cvt_pk_bf16_f32 v210, v84, v85\n\t"
  "v_cvt_pk_bf16_f32 v211, v86, v87\n\t"
  "v_cvt_pk_bf16_f32 v236, v88, v89\n\t"
  "v_cvt_pk_bf16_f32 v237, v90, v91\n\t"
  "v_cvt_pk_bf16_f32 v246, v92, v93\n\t"
  "v_cvt_pk_bf16_f32 v247, v94, v95\n\t"
  "ds_write_b128 v252, v[208:211] offset:2048\n\t"
  "ds_write_b64 v252, v[236:237] offset:3072\n\t"
  "ds_write_b64 v252, v[246:247] offset:3080\n\t"
  "v_cvt_pk_bf16_f32 v212, v176, v177\n\t"
  "v_cvt_pk_bf16_f32 v213, v178, v179\n\t"
  "v_cvt_pk_bf16_f32 v214, v180, v181\n\t"
  "v_cvt_pk_bf16_f32 v215, v182, v183\n\t"
  "v_cvt_pk_bf16_f32 v216, v184, v185\n\t"
  "v_cvt_pk_bf16_f32 v217, v186, v187\n\t"
  "v_cvt_pk_bf16_f32 v218, v188, v189\n\t"
  "v_cvt_pk_bf16_f32 v219, v190, v191\n\t"
  "ds_write_b128 v252, v[212:215] offset:12288\n\t"
  "ds_write_b128 v252, v[216:219] offset:13312\n\t"
  "ds_read_b128 v[92:95], v253\n\t"
  "v_cvt_pk_bf16_f32 v224, v72, v73\n\t"
  "v_cvt_pk_bf16_f32 v225, v74, v75\n\t"
  "v_cvt_pk_bf16_f32 v226, v76, v77\n\t"
  "v_cvt_pk_bf16_f32 v227, v78, v79\n\t"
  "v_cvt_pk_bf16_f32 v204, v56, v57\n\t"
  "v_cvt_pk_bf16_f32 v205, v58, v59\n\t"
  "v_cvt_pk_bf16_f32 v206, v60, v61\n\t"
  "v_cvt_pk_bf16_f32 v207, v62, v63\n\t"
  "v_mfma_f32_16x16x32_bf16 v[72:75], v[208:211], v[224:227], 0\n\t"
  "v_cvt_pk_bf16_f32 v220, v48, v49\n\t"
  "v_cvt_pk_bf16_f32 v221, v50, v51\n\t"
  "v_cvt_pk_bf16_f32 v222, v52, v53\n\t"
  "v_cvt_pk_bf16_f32 v223, v54, v55\n\t"
  "v_mfma_f32_16x16x32_bf16 v[76:79], v[204:207], v[224:227], 0\n\t"
  "v_cvt_pk_bf16_f32 v200, v32, v33\n\t"
  "v_cvt_pk_bf16_f32 v201, v34, v35\n\t"
  "v_cvt_pk_bf16_f32 v202, v36, v37\n\t"
  "v_cvt_pk_bf16_f32 v203, v38, v39\n\t"
  "v_mfma_f32_16x16x32_bf16 v[48:51], v[204:207], v[220:223], 0\n\t"
  "s_waitcnt lgkmcnt(0)\n\t"
  "s_barrier\n\t"
  "ds_read_b128 v[192:195], v252 offset:0\n\t"
  "ds_read_b64 v[228:229], v252 offset:1024\n\t"
  "ds_read_b64 v[238:239], v252 offset:1032\n\t"
  "v_cvt_pk_bf16_f32 v216, v24, v25\n\t"
  "v_cvt_pk_bf16_f32 v217, v26, v27\n\t"
  "v_cvt_pk_bf16_f32 v218, v28, v29\n\t"
  "v_cvt_pk_bf16_f32 v219, v30, v31\n\t"
  "v_mfma_f32_16x16x32_bf16 v[52:55], v[200:203], v[220:223], 0\n\t"
  "v_cvt_pk_bf16_f32 v196, v8, v9\n\t"
  "v_cvt_pk_bf16_f32 v197, v10, v11\n\t"
  "v_cvt_pk_bf16_f32 v198, v12, v13\n\t"
  "v_cvt_pk_bf16_f32 v199, v14, v15\n\t"
  "v_mfma_f32_16x16x32_bf16 v[24:27], v[200:203], v[216:219], 0\n\t"
  "v_cvt_pk_bf16_f32 v212, v0, v1\n\t"
  "v_cvt_pk_bf16_f32 v213, v2, v3\n\t"
  "v_cvt_pk_bf16_f32 v214, v4, v5\n\t"
  "v_cvt_pk_bf16_f32 v215, v6, v7\n\t"
  "v_mfma_f32_16x16x32_bf16 v[28:31], v[196:199], v[216:219], 0\n\t"
  "v_cndmask_b32_e64 v80, v72, v73, %[par]\n\t"
  "v_cndmask_b32_e64 v81, v76, v77, %[par]\n\t"
  "v_cndmask_b32_e64 v82, v74, v75, %[par]\n\t"
  "v_mfma_f32_16x16x32_bf16 v[0:3], v[196:199], v[212:215], 0\n\t"
  "v_cndmask_b32_e64 v83, v78, v79, %[par]\n\t"
  "v_cndmask_b32_e64 v80, v81, v80, %[c0]\n\t"
  "v_cndmask_b32_e64 v81, v83, v82, %[c1]\n\t"
  "v_max_f32 v82, v80, v81\n\t"
  "s_waitcnt lgkmcnt(0)\n\t"
  "v_mfma_f32_16x16x32_bf16 v[4:7], v[192:195], v[212:215], 0\n\t"
  "v_cndmask_b32_e64 v56, v48, v49, %[par]\n\t"
  "v_cndmask_b32_e64 v57, v52, v53, %[par]\n\t"
  "v_cndmask_b32_e64 v58, v50, v51, %[par]\n\t"
  "v_cndmask_b32_e64 v59, v54, v55, %[par]\n\t"
  "v_cndmask_b32_e64 v56, v57, v56, %[c0]\n\t"
  "v_cndmask_b32_e64 v57, v59, v58, %[c1]\n\t"
  "v_max_f32 v58, v56, v57\n\t"
  "v_cndmask_b32_e64 v32, v24, v25, %[par]\n\t"
  "v_cndmask_b32_e64 v33, v28, v29, %[par]\n\t"
  "v_cndmask_b32_e64 v34, v26, v27, %[par]\n\t"
  "v_cndmask_b32_e64 v35, v30, v31, %[par]\n\t"
  "v_cndmask_b32_e64 v32, v33, v32, %[c0]\n\t"
  "v_cndmask_b32_e64 v33, v35, v34, %[c1]\n\t"
  "v_max_f32 v34, v32, v33\n\t"
  "v_mov_b32 v15, 0xff800000\n\t"
  "v_permlane16_swap_b32 v58, v82\n\t"
  "v_cndmask_b32_e64 v8, v0, v1, %[par]\n\t"
  "v_cndmask_b32_e64 v9, v4, v5, %[par]\n\t"
  "v_cndmask_b32_e64 v10, v2, v3, %[par]\n\t"
  "v_cndmask_b32_e64 v11, v6, v7, %[par]\n\t"
  "v_cndmask_b32_e64 v9, v9, v15, %[np]\n\t"
  "v_cndmask_b32_e64 v11, v11, v15, %[np]\n\t"
  "v_cndmask_b32_e64 v8, v9, v8, %[c0]\n\t"
  "v_cndmask_b32_e64 v9, v11, v10, %[c1]\n\t"
  "v_max_f32 v10, v8, v9\n\t"
  "v_max_f32 v58, v58, v82\n\t"
  "v_cvt_pk_bf16_f32 v234, v64, v65\n\t"
  "v_cvt_pk_bf16_f32 v235, v66, v67\n\t"
  "v_permlane16_swap_b32 v10, v34\n\t"
  "v_cvt_pk_bf16_f32 v244, v68, v69\n\t"
  "v_max_f32 v10, v10, v34\n\t"
  "v_cvt_pk_bf16_f32 v245, v70, v71\n\t"
  "v_cvt_pk_bf16_f32 v232, v40, v41\n\t"
  "v_permlane32_swap_b32 v10, v58\n\t"
  "v_cvt_pk_bf16_f32 v233, v42, v43\n\t"
  "v_max_f32 v10, v10, v58\n\t"
  "v_cvt_pk_bf16_f32 v242, v44, v45\n\t"
  "v_mul_f32 v10, %[sc], v10\n\t"
  "v_cvt_pk_bf16_f32 v243, v46, v47\n\t"
  "v_mov_b32 v58, v10\n\t"
  "v_cvt_pk_bf16_f32 v230, v16, v17\n\t"
  "v_cvt_pk_bf16_f32 v231, v18, v19\n\t"
  "v_permlane32_swap_b32 v10, v58\n\t"
  "v_cvt_pk_bf16_f32 v240, v20, v21\n\t"
  "v_mov_b32 v34, v10\n\t"
  "v_mov_b32 v82, v58\n\t"
  "v_cvt_pk_bf16_f32 v241, v22, v23\n\t"
  "v_mov_b32 v192, 0x3f803f80\n\t"
  "v_permlane16_swap_b32 v10, v34\n\t"
  "v_permlane16_swap_b32 v58, v82\n\t"
  "v_mov_b32 v193, 0x3f803f80\n\t"
  "v_mov_b32 v194, 0x3f803f80\n\t"
  "v_mov_b32 v195, 0x3f803f80\n\t"
  "v_fma_f32 v8, v8, %[sc], -v10\n\t"
  "v_fma_f32 v9, v9, %[sc], -v10\n\t"
  "v_fma_f32 v32, v32, %[sc], -v34\n\t"
  "v_fma_f32 v33, v33, %[sc], -v34\n\t"
  "v_fma_f32 v56, v56, %[sc], -v58\n\t"
  "v_fma_f32 v57, v57, %[sc], -v58\n\t"
  "v_fma_f32 v80, v80, %[sc], -v82\n\t"
  "v_fma_f32 v81, v81, %[sc], -v82\n\t"
  "v_exp_f32 v8, v8\n\t"
  "v_exp_f32 v9, v9\n\t"
  "v_exp_f32 v32, v32\n\t"
  "v_exp_f32 v33, v33\n\t"
  "v_exp_f32 v56, v56\n\t"
  "v_exp_f32 v57, v57\n\t"
  "v_exp_f32 v80, v80\n\t"
  "v_exp_f32 v81, v81\n\t"
  "v_cvt_pk_bf16_f32 v8, v8, v9\n\t"
  "v_cvt_pk_bf16_f32 v32, v32, v33\n\t"
  "v_cvt_pk_bf16_f32 v56, v56, v57\n\t"
  "v_cvt_pk_bf16_f32 v80, v80, v81\n\t"
  "v_perm_b32 v12, v8, v8, v248\n\t"
  "v_perm_b32 v13, v8, v8, v249\n\t"
  "v_perm_b32 v14, v8, v8, v250\n\t"
  "v_perm_b32 v15, v8, v8, v251\n\t"
  "v_perm_b32 v36, v32, v32, v248\n\t"
  "v_perm_b32 v37, v32, v32, v249\n\t"
  "v_perm_b32 v38, v32, v32, v250\n\t"
  "v_perm_b32 v39, v32, v32, v251\n\t"
  "v_mfma_f32_16x16x32_bf16 v[16:19], v[192:195], v[12:15], 0\n\t"
  "v_perm_b32 v60, v56, v56, v248\n\t"
  "v_perm_b32 v61, v56, v56, v249\n\t"
  "v_mfma_f32_16x16x32_bf16 v[0:3], v[228:231], v[12:15], 0\n\t"
  "v_perm_b32 v62, v56, v56, v250\n\t"
  "v_mfma_f32_16x16x32_bf16 v[4:7], v[238:241], v[12:15], 0\n\t"
  "v_perm_b32 v63, v56, v56, v251\n\t"
  "v_mfma_f32_16x16x32_bf16 v[40:43], v[192:195], v[36:39], 0\n\t"
  "v_perm_b32 v84, v80, v80, v248\n\t"
  "v_perm_b32 v85, v80, v80, v249\n\t"
  "v_mfma_f32_16x16x32_bf16 v[24:27], v[230:233], v[36:39], 0\n\t"
  "v_perm_b32 v86, v80, v80, v250\n\t"
  "v_mfma_f32_16x16x32_bf16 v[28:31], v[240:243], v[36:39], 0\n\t"
  "v_perm_b32 v87, v80, v80, v251\n\t"
  "v_mfma_f32_16x16x32_bf16 v[64:67], v[192:195], v[60:63], 0\n\t"
  "v_rcp_f32 v16, v16\n\t"
  "s_nop 0\n\t"
  "v_mul_f32 v16, v16, v92\n\t"
  "v_mul_f32 v0, v0, v16\n\t"
  "v_mfma_f32_16x16x32_bf16 v[48:51], v[232:235], v[60:63], 0\n\t"
  "v_mul_f32 v1, v1, v16\n\t"
  "v_mul_f32 v2, v2, v16\n\t"
  "v_mfma_f32_16x16x32_bf16 v[52:55], v[242:245], v[60:63], 0\n\t"
  "v_mul_f32 v3, v3, v16\n\t"
  "v_mul_f32 v4, v4, v16\n\t"
  "v_mul_f32 v5, v5, v16\n\t"
  "v_mfma_f32_16x16x32_bf16 v[88:91], v[192:195], v[84:87], 0\n\t"
  "v_mul_f32 v6, v6, v16\n\t"
  "v_mul_f32 v7, v7, v16\n\t"
  "v_mfma_f32_16x16x32_bf16 v[72:75], v[234:237], v[84:87], 0\n\t"
  "v_cvt_pk_bf16_f32 v12, v0, v1\n\t"
  "v_cvt_pk_bf16_f32 v13, v2, v3\n\t"
  "v_cvt_pk_bf16_f32 v14, v4, v5\n\t"
  "v_mfma_f32_16x16x32_bf16 v[76:79], v[244:247], v[84:87], 0\n\t"
  "v_cvt_pk_bf16_f32 v15, v6, v7\n\t"
  "buffer_store_dwordx4 v[12:15], v252, %[srd], %[so0] offen offset:0 sc1\n\t"
  "v_rcp_f32 v40, v40\n\t"
  "s_nop 0\n\t"
  "v_mul_f32 v40, v40, v93\n\t"
  "v_mul_f32 v24, v24, v40\n\t"
  "v_mul_f32 v25, v25, v40\n\t"
  "v_mul_f32 v26, v26, v40\n\t"
  "v_mul_f32 v27, v27, v40\n\t"
  "v_mul_f32 v28, v28, v40\n\t"
  "v_mul_f32 v29, v29, v40\n\t"
  "v_mul_f32 v30, v30, v40\n\t"
  "v_mul_f32 v31, v31, v40\n\t"
  "v_cvt_pk_bf16_f32 v36, v24, v25\n\t"
  "v_cvt_pk_bf16_f32 v37, v26, v27\n\t"
  "v_cvt_pk_bf16_f32 v38, v28, v29\n\t"
  "v_cvt_pk_bf16_f32 v39, v30, v31\n\t"
  "buffer_store_dwordx4 v[36:39], v252, %[srd], %[so0] offen offset:2048 sc1\n\t"
  "v_rcp_f32 v64, v64\n\t"
  "s_nop 0\n\t"
  "v_mul_f32 v64, v64, v94\n\t"
  "v_mul_f32 v48, v48, v64\n\t"
  "v_mul_f32 v49, v49, v64\n\t"
  "v_mul_f32 v50, v50, v64\n\t"
  "v_mul_f32 v51, v51, v64\n\t"
  "v_mul_f32 v52, v52, v64\n\t"
  "v_mul_f32 v53, v53, v64\n\t"
  "v_mul_f32 v54, v54, v64\n\t"
  "v_mul_f32 v55, v55, v64\n\t"
  "v_cvt_pk_bf16_f32 v60, v48, v49\n\t"
  "v_cvt_pk_bf16_f32 v61, v50, v51\n\t"
  "v_cvt_pk_bf16_f32 v62, v52, v53\n\t"
  "v_cvt_pk_bf16_f32 v63, v54, v55\n\t"
  "buffer_store_dwordx4 v[60:63], v252, %[srd], %[so1] offen offset:0 sc1\n\t"
  "v_rcp_f32 v88, v88\n\t"
  "s_nop 0\n\t"
  "v_mul_f32 v88, v88, v95\n\t"
  "v_mul_f32 v72, v72, v88\n\t"
  "v_mul_f32 v73, v73, v88\n\t"
  "v_mul_f32 v74, v74, v88\n\t"
  "v_mul_f32 v75, v75, v88\n\t"
  "v_mul_f32 v76, v76, v88\n\t"
  "v_mul_f32 v77, v77, v88\n\t"
  "v_mul_f32 v78, v78, v88\n\t"
  "v_mul_f32 v79, v79, v88\n\t"
  "v_cvt_pk_bf16_f32 v84, v72, v73\n\t"
  "v_cvt_pk_bf16_f32 v85, v74, v75\n\t"
  "v_cvt_pk_bf16_f32 v86, v76, v77\n\t"
  "v_cvt_pk_bf16_f32 v87, v78, v79\n\t"
  "buffer_store_dwordx4 v[84:87], v252, %[srd], %[so1] offen offset:2048 sc1\n\t"
  "v_cvt_pk_bf16_f32 v208, v176, v177\n\t"
  "v_cvt_pk_bf16_f32 v209, v178, v179\n\t"
  "v_cvt_pk_bf16_f32 v210, v180, v181\n\t"
  "v_cvt_pk_bf16_f32 v211, v182, v183\n\t"
  "v_cvt_pk_bf16_f32 v236, v184, v185\n\t"
  "v_cvt_pk_bf16_f32 v237, v186, v187\n\t"
  "v_cvt_pk_bf16_f32 v246, v188, v189\n\t"
  "v_cvt_pk_bf16_f32 v247, v190, v191\n\t"
  "ds_read_b128 v[192:195], v252 offset:10240\n\t"
  "ds_read_b64 v[228:229], v252 offset:11264\n\t"
  "ds_read_b64 v[238:239], v252 offset:11272\n\t"
  "v_cvt_pk_bf16_f32 v224, v168, v169\n\t"
  "v_cvt_pk_bf16_f32 v225, v170, v171\n\t"
  "v_cvt_pk_bf16_f32 v226, v172, v173\n\t"
  "v_cvt_pk_bf16_f32 v227, v174, v175\n\t"
  "v_cvt_pk_bf16_f32 v204, v152, v153\n\t"
  "v_cvt_pk_bf16_f32 v205, v154, v155\n\t"
  "v_cvt_pk_bf16_f32 v206, v156, v157\n\t"
  "v_cvt_pk_bf16_f32 v207, v158, v159\n\t"
  "v_mfma_f32_16x16x32_bf16 v[168:171], v[208:211], v[224:227], 0\n\t"
  "v_cvt_pk_bf16_f32 v220, v144, v145\n\t"
  "v_cvt_pk_bf16_f32 v221, v146, v147\n\t"
  "v_cvt_pk_bf16_f32 v222, v148, v149\n\t"
  "v_cvt_pk_bf16_f32 v223, v150, v151\n\t"
  "v_mfma_f32_16x16x32_bf16 v[172:175], v[204:207], v[224:227], 0\n\t"
  "v_cvt_pk_bf16_f32 v200, v128, v129\n\t"
  "v_cvt_pk_bf16_f32 v201, v130, v131\n\t"
  "v_cvt_pk_bf16_f32 v202, v132, v133\n\t"
  "v_cvt_pk_bf16_f32 v203, v134, v135\n\t"
  "v_mfma_f32_16x16x32_bf16 v[144:147], v[204:207], v[220:223], 0\n\t"
  "v_cvt_pk_bf16_f32 v216, v120, v121\n\t"
  "v_cvt_pk_bf16_f32 v217, v122, v123\n\t"
  "v_cvt_pk_bf16_f32 v218, v124, v125\n\t"
  "v_cvt_pk_bf16_f32 v219, v126, v127\n\t"
  "v_mfma_f32_16x16x32_bf16 v[148:151], v[200:203], v[220:223], 0\n\t"
  "v_cvt_pk_bf16_f32 v196, v104, v105\n\t"
  "v_cvt_pk_bf16_f32 v197, v106, v107\n\t"
  "v_cvt_pk_bf16_f32 v198, v108, v109\n\t"
  "v_cvt_pk_bf16_f32 v199, v110, v111\n\t"
  "v_mfma_f32_16x16x32_bf16 v[120:123], v[200:203], v[216:219], 0\n\t"
  "v_cvt_pk_bf16_f32 v212, v96, v97\n\t"
  "v_cvt_pk_bf16_f32 v213, v98, v99\n\t"
  "v_cvt_pk_bf16_f32 v214, v100, v101\n\t"
  "v_cvt_pk_bf16_f32 v215, v102, v103\n\t"
  "v_mfma_f32_16x16x32_bf16 v[124:127], v[196:199], v[216:219], 0\n\t"
  "v_cndmask_b32_e64 v176, v168, v169, %[par]\n\t"
  "v_cndmask_b32_e64 v177, v172, v173, %[par]\n\t"
  "v_cndmask_b32_e64 v178, v170, v171, %[par]\n\t"
  "v_mfma_f32_16x16x32_bf16 v[96:99], v[196:199], v[212:215], 0\n\t"
  "v_cndmask_b32_e64 v179, v174, v175, %[par]\n\t"
  "v_cndmask_b32_e64 v176, v177, v176, %[c0]\n\t"
  "v_cndmask_b32_e64 v177, v179, v178, %[c1]\n\t"
  "v_max_f32 v178, v176, v177\n\t"
  "s_waitcnt lgkmcnt(0)\n\t"
  "v_mfma_f32_16x16x32_bf16 v[100:103], v[192:195], v[212:215], 0\n\t"
  "v_cndmask_b32_e64 v152, v144, v145, %[par]\n\t"
  "v_cndmask_b32_e64 v153, v148, v149, %[par]\n\t"
  "v_cndmask_b32_e64 v154, v146, v147, %[par]\n\t"
  "v_cndmask_b32_e64 v155, v150, v151, %[par]\n\t"
  "v_cndmask_b32_e64 v152, v153, v152, %[c0]\n\t"
  "v_cndmask_b32_e64 v153, v155, v154, %[c1]\n\t"
  "v_max_f32 v154, v152, v153\n\t"
  "v_cndmask_b32_e64 v128, v120, v121, %[par]\n\t"
  "v_cndmask_b32_e64 v129, v124, v125, %[par]\n\t"
  "v_cndmask_b32_e64 v130, v122, v123, %[par]\n\t"
  "v_cndmask_b32_e64 v131, v126, v127, %[par]\n\t"
  "v_cndmask_b32_e64 v128, v129, v128, %[c0]\n\t"
  "v_cndmask_b32_e64 v129, v131, v130, %[c1]\n\t"
  "v_max_f32 v130, v128, v129\n\t"
  "v_mov_b32 v111, 0xff800000\n\t"
  "v_permlane16_swap_b32 v154, v178\n\t"
  "v_cndmask_b32_e64 v104, v96, v97, %[par]\n\t"
  "v_cndmask_b32_e64 v105, v100, v101, %[par]\n\t"
  "v_cndmask_b32_e64 v106, v98, v99, %[par]\n\t"
  "v_cndmask_b32_e64 v107, v102, v103, %[par]\n\t"
  "v_cndmask_b32_e64 v105, v105, v111, %[np]\n\t"
  "v_cndmask_b32_e64 v107, v107, v111, %[np]\n\t"
  "v_cndmask_b32_e64 v104, v105, v104, %[c0]\n\t"
  "v_cndmask_b32_e64 v105, v107, v106, %[c1]\n\t"
  "v_max_f32 v106, v104, v105\n\t"
  "v_max_f32 v154, v154, v178\n\t"
  "v_cvt_pk_bf16_f32 v234, v160, v161\n\t"
  "v_cvt_pk_bf16_f32 v235, v162, v163\n\t"
  "v_permlane16_swap_b32 v106, v130\n\t"
  "v_cvt_pk_bf16_f32 v244, v164, v165\n\t"
  "v_max_f32 v106, v106, v130\n\t"
  "v_cvt_pk_bf16_f32 v245, v166, v167\n\t"
  "v_cvt_pk_bf16_f32 v232, v136, v137\n\t"
  "v_permlane32_swap_b32 v106, v154\n\t"
  "v_cvt_pk_bf16_f32 v233, v138, v139\n\t"
  "v_max_f32 v106, v106, v154\n\t"
  "v_cvt_pk_bf16_f32 v242, v140, v141\n\t"
  "v_mul_f32 v106, %[sc], v106\n\t"
  "v_cvt_pk_bf16_f32 v243, v142, v143\n\t"
  "v_mov_b32 v154, v106\n\t"
  "v_cvt_pk_bf16_f32 v230, v112, v113\n\t"
  "v_cvt_pk_bf16_f32 v231, v114, v115\n\t"
  "v_permlane32_swap_b32 v106, v154\n\t"
  "v_cvt_pk_bf16_f32 v240, v116, v117\n\t"
  "v_mov_b32 v130, v106\n\t"
  "v_mov_b32 v178, v154\n\t"
  "v_cvt_pk_bf16_f32 v241, v118, v119\n\t"
  "v_mov_b32 v192, 0x3f803f80\n\t"
  "v_permlane16_swap_b32 v106, v130\n\t"
  "v_permlane16_swap_b32 v154, v178\n\t"
  "v_mov_b32 v193, 0x3f803f80\n\t"
  "v_mov_b32 v194, 0x3f803f80\n\t"
  "v_mov_b32 v195, 0x3f803f80\n\t"
  "v_fma_f32 v104, v104, %[sc], -v106\n\t"
  "v_fma_f32 v105, v105, %[sc], -v106\n\t"
  "v_fma_f32 v128, v128, %[sc], -v130\n\t"
  "v_fma_f32 v129, v129, %[sc], -v130\n\t"
  "v_fma_f32 v152, v152, %[sc], -v154\n\t"
  "v_fma_f32 v153, v153, %[sc], -v154\n\t"
  "v_fma_f32 v176, v176, %[sc], -v178\n\t"
  "v_fma_f32 v177, v177, %[sc], -v178\n\t"
  "v_exp_f32 v104, v104\n\t"
  "v_exp_f32 v105, v105\n\t"
  "v_exp_f32 v128, v128\n\t"
  "v_exp_f32 v129, v129\n\t"
  "v_exp_f32 v152, v152\n\t"
  "v_exp_f32 v153, v153\n\t"
  "v_exp_f32 v176, v176\n\t"
  "v_exp_f32 v177, v177\n\t"
  "v_cvt_pk_bf16_f32 v104, v104, v105\n\t"
  "v_cvt_pk_bf16_f32 v128, v128, v129\n\t"
  "v_cvt_pk_bf16_f32 v152, v152, v153\n\t"
  "v_cvt_pk_bf16_f32 v176, v176, v177\n\t"
  "v_perm_b32 v108, v104, v104, v248\n\t"
  "v_perm_b32 v109, v104, v104, v249\n\t"
  "v_perm_b32 v110, v104, v104, v250\n\t"
  "v_perm_b32 v111, v104, v104, v251\n\t"
  "v_perm_b32 v132, v128, v128, v248\n\t"
  "v_perm_b32 v133, v128, v128, v249\n\t"
  "v_perm_b32 v134, v128, v128, v250\n\t"
  "v_perm_b32 v135, v128, v128, v251\n\t"
  "v_mfma_f32_16x16x32_bf16 v[112:115], v[192:195], v[108:111], 0\n\t"
  "v_perm_b32 v156, v152, v152, v248\n\t"
  "v_perm_b32 v157, v152, v152, v249\n\t"
  "v_mfma_f32_16x16x32_bf16 v[96:99], v[228:231], v[108:111], 0\n\t"
  "v_perm_b32 v158, v152, v152, v250\n\t"
  "v_mfma_f32_16x16x32_bf16 v[100:103], v[238:241], v[108:111], 0\n\t"
  "v_perm_b32 v159, v152, v152, v251\n\t"
  "v_mfma_f32_16x16x32_bf16 v[136:139], v[192:195], v[132:135], 0\n\t"
  "v_perm_b32 v180, v176, v176, v248\n\t"
  "v_perm_b32 v181, v176, v176, v249\n\t"
  "v_mfma_f32_16x16x32_bf16 v[120:123], v[230:233], v[132:135], 0\n\t"
  "v_perm_b32 v182, v176, v176, v250\n\t"
  "v_mfma_f32_16x16x32_bf16 v[124:127], v[240:243], v[132:135], 0\n\t"
  "v_perm_b32 v183, v176, v176, v251\n\t"
  "v_mfma_f32_16x16x32_bf16 v[160:163], v[192:195], v[156:159], 0\n\t"
  "v_rcp_f32 v112, v112\n\t"
  "s_nop 0\n\t"
  "v_mul_f32 v112, v112, v92\n\t"
  "v_mul_f32 v96, v96, v112\n\t"
  "v_mfma_f32_16x16x32_bf16 v[144:147], v[232:235], v[156:159], 0\n\t"
  "v_mul_f32 v97, v97, v112\n\t"
  "v_mul_f32 v98, v98, v112\n\t"
  "v_mfma_f32_16x16x32_bf16 v[148:151], v[242:245], v[156:159], 0\n\t"
  "v_mul_f32 v99, v99, v112\n\t"
  "v_mul_f32 v100, v100, v112\n\t"
  "v_mul_f32 v101, v101, v112\n\t"
  "v_mfma_f32_16x16x32_bf16 v[184:187], v[192:195], v[180:183], 0\n\t"
  "v_mul_f32 v102, v102, v112\n\t"
  "v_mul_f32 v103, v103, v112\n\t"
  "v_mfma_f32_16x16x32_bf16 v[168:171], v[234:237], v[180:183], 0\n\t"
  "v_cvt_pk_bf16_f32 v108, v96, v97\n\t"
  "v_cvt_pk_bf16_f32 v109, v98, v99\n\t"
  "v_cvt_pk_bf16_f32 v110, v100, v101\n\t"
  "v_mfma_f32_16x16x32_bf16 v[172:175], v[244:247], v[180:183], 0\n\t"
  "v_cvt_pk_bf16_f32 v111, v102, v103\n\t"
  "buffer_store_dwordx4 v[108:111], v252, %[srd], %[so0] offen offset:1024 sc1\n\t"
  "v_rcp_f32 v136, v136\n\t"
  "s_nop 0\n\t"
  "v_mul_f32 v136, v136, v93\n\t"
  "v_mul_f32 v120, v120, v136\n\t"
  "v_mul_f32 v121, v121, v136\n\t"
  "v_mul_f32 v122, v122, v136\n\t"
  "v_mul_f32 v123, v123, v136\n\t"
  "v_mul_f32 v124, v124, v136\n\t"
  "v_mul_f32 v125, v125, v136\n\t"
  "v_mul_f32 v126, v126, v136\n\t"
  "v_mul_f32 v127, v127, v136\n\t"
  "v_cvt_pk_bf16_f32 v132, v120, v121\n\t"
  "v_cvt_pk_bf16_f32 v133, v122, v123\n\t"
  "v_cvt_pk_bf16_f32 v134, v124, v125\n\t"
  "v_cvt_pk_bf16_f32 v135, v126, v127\n\t"
  "buffer_store_dwordx4 v[132:135], v252, %[srd], %[so0] offen offset:3072 sc1\n\t"
  "v_rcp_f32 v160, v160\n\t"
  "s_nop 0\n\t"
  "v_mul_f32 v160, v160, v94\n\t"
  "v_mul_f32 v144, v144, v160\n\t"
  "v_mul_f32 v145, v145, v160\n\t"
  "v_mul_f32 v146, v146, v160\n\t"
  "v_mul_f32 v147, v147, v160\n\t"
  "v_mul_f32 v148, v148, v160\n\t"
  "v_mul_f32 v149, v149, v160\n\t"
  "v_mul_f32 v150, v150, v160\n\t"
  "v_mul_f32 v151, v151, v160\n\t"
  "v_cvt_pk_bf16_f32 v156, v144, v145\n\t"
  "v_cvt_pk_bf16_f32 v157, v146, v147\n\t"
  "v_cvt_pk_bf16_f32 v158, v148, v149\n\t"
  "v_cvt_pk_bf16_f32 v159, v150, v151\n\t"
  "buffer_store_dwordx4 v[156:159], v252, %[srd], %[so1] offen offset:1024 sc1\n\t"
  "v_rcp_f32 v184, v184\n\t"
  "s_nop 0\n\t"
  "v_mul_f32 v184, v184, v95\n\t"
  "v_mul_f32 v168, v168, v184\n\t"
  "v_mul_f32 v169, v169, v184\n\t"
  "v_mul_f32 v170, v170, v184\n\t"
  "v_mul_f32 v171, v171, v184\n\t"
  "v_mul_f32 v172, v172, v184\n\t"
  "v_mul_f32 v173, v173, v184\n\t"
  "v_mul_f32 v174, v174, v184\n\t"
  "v_mul_f32 v175, v175, v184\n\t"
  "v_cvt_pk_bf16_f32 v180, v168, v169\n\t"
  "v_cvt_pk_bf16_f32 v181, v170, v171\n\t"
  "v_cvt_pk_bf16_f32 v182, v172, v173\n\t"
  "v_cvt_pk_bf16_f32 v183, v174, v175\n\t"
  "buffer_store_dwordx4 v[180:183], v252, %[srd], %[so1] offen offset:3072 sc1\n\t"
  "s_nop 1\n\t"
  :: "{v[0:31]}"(ac[0]), "{v[32:63]}"(ac[1]), "{v[64:95]}"(ac[2]), "{v[96:127]}"(ac[3]), "{v[128:159]}"(ac[4]), "{v[160:191]}"(ac[5]), "{v248}"(ps0), "{v249}"(ps1), "{v250}"(ps2), "{v251}"(ps3), "{v252}"(xr), "{v253}"(ga), [par]"s"(mpar), [c0]"s"(mc0), [c1]"s"(mc1), [np]"s"(mnp), [srd]"s"(sO), [so0]"s"(so0), [so1]"s"(so0 + 4096), [sc]"s"(0.17677669529663687f * 1.4426950408889634f) : "memory","v192","v193","v194","v195","v196","v197","v198","v199","v200","v201","v202","v203","v204","v205","v206","v207","v208","v209","v210","v211","v212","v213","v214","v215","v216","v217","v218","v219","v220","v221","v222","v223","v224","v225","v226","v227","v228","v229","v230","v231","v232","v233","v234","v235","v236","v237","v238","v239","v240","v241","v242","v243","v244","v245","v246","v247");
    }
    {
      LANES;
      const unsigned sa = wm * 8192 + lane * 16;
      const unsigned sbr = L_SB + (wn * 64 + l15) * 128 + ((g ^ (lane & 7)) << 4);
      const unsigned sbt = L_SB + wn * 8192 + (4 * g + q) * 128 + (f << 5) + p * 8;
      const int ntr = (NTR + 2) >> 2, nrow = (T2 - NTR + 1) >> 1, isp = so;
      const unsigned one = g == (l15 >> 2) ? (l15 & 1 ? 0x3f800000u : 0x00003f80u) : 0u, ea = l15 & 2 ? 0u : one, eb = l15 & 2 ? one : 0u;
      const u32x4 e0 = {ea, eb, 0u, 0u}, e1 = {0u, 0u, ea, eb};
      const unsigned pvb = (wid < 2 ? PART_A + wid * 8192 : PART_B + (wid - 2) * 8192) + lane * 16, vo = (lane >> 3) * 2048 + (lane & 7) * 16;
      const unsigned tw0 = L_X + wid * 8192 + l15 * 128 + (((g >> 1) ^ (lane & 7)) << 4) + (g & 1) * 8;
      const unsigned trb = L_X + wid * 8192 + (lane >> 3) * 128 + (((lane & 7) ^ (lane >> 3)) << 4);
      const i32x4 sOut = mksrd(out + (size_t)(tm * 128 + wm * 64) * 1024 + tn * 128 + wn * 64, 64 * 2048 - (tn * 128 + wn * 64) * 2);
      const i32x4 sPt = mksrd(ws + WS_PART + (size_t)(tm * 8 + tn) * 32768, 32768);
      const u64 tag = *(LAS u64*)(size_t)L_TAG;
      char* const pflag = ws + WS_MB + (128 + tm * 8 + tn) * 256 + 192 + wid * 8;
      asm volatile(
  "v_mov_b64 v[0:1], 0\n\t"
  "v_mov_b64 v[2:3], 0\n\t"
  "v_mov_b64 v[4:5], 0\n\t"
  "v_mov_b64 v[6:7], 0\n\t"
  "v_mov_b64 v[8:9], 0\n\t"
  "v_mov_b64 v[10:11], 0\n\t"
  "v_mov_b64 v[12:13], 0\n\t"
  "v_mov_b64 v[14:15], 0\n\t"
  "v_mov_b64 v[16:17], 0\n\t"
  "v_mov_b64 v[18:19], 0\n\t"
  "v_mov_b64 v[20:21], 0\n\t"
  "v_mov_b64 v[22:23], 0\n\t"
  "v_mov_b64 v[24:25], 0\n\t"
  "v_mov_b64 v[26:27], 0\n\t"
  "v_mov_b64 v[28:29], 0\n\t"
  "v_mov_b64 v[30:31], 0\n\t"
  "v_mov_b64 v[32:33], 0\n\t"
  "v_mov_b64 v[34:35], 0\n\t"
  "v_mov_b64 v[36:37], 0\n\t"
  "v_mov_b64 v[38:39], 0\n\t"
  "v_mov_b64 v[40:41], 0\n\t"
  "v_mov_b64 v[42:43], 0\n\t"
  "v_mov_b64 v[44:45], 0\n\t"
  "v_mov_b64 v[46:47], 0\n\t"
  "v_mov_b64 v[48:49], 0\n\t"
  "v_mov_b64 v[50:51], 0\n\t"
  "v_mov_b64 v[52:53], 0\n\t"
  "v_mov_b64 v[54:55], 0\n\t"
  "v_mov_b64 v[56:57], 0\n\t"
  "v_mov_b64 v[58:59], 0\n\t"
  "v_mov_b64 v[60:61], 0\n\t"
  "v_mov_b64 v[62:63], 0\n\t"
  "s_waitcnt vmcnt(0)\n\t"
  "s_barrier\n\t"
  "s_barrier\n\t"
  "ds_read_b128 v[64:67], v128 offset:32768\n\t"
  "ds_read_b128 v[68:71], v128 offset:33792\n\t"
  "ds_read_b64_tr_b16 v[96:97], v129 offset:16384\n\t"
  "ds_read_b64_tr_b16 v[98:99], v129 offset:18432\n\t"
  "ds_read_b64_tr_b16 v[100:101], v129 offset:20480\n\t"
  "ds_read_b64_tr_b16 v[102:103], v129 offset:22528\n\t"
  "ds_read_b128 v[72:75], v128 offset:34816\n\t"
  "ds_read_b128 v[76:79], v128 offset:35840\n\t"
  "ds_read_b64_tr_b16 v[104:105], v130 offset:16384\n\t"
  "ds_read_b64_tr_b16 v[106:107], v130 offset:18432\n\t"
  "ds_read_b64_tr_b16 v[108:109], v130 offset:20480\n\t"
  "ds_read_b64_tr_b16 v[110:111], v130 offset:22528\n\t"
  "ds_read_b64_tr_b16 v[112:113], v131 offset:16384\n\t"
  "ds_read_b64_tr_b16 v[114:115], v131 offset:18432\n\t"
  "ds_read_b64_tr_b16 v[116:117], v131 offset:20480\n\t"
  "ds_read_b64_tr_b16 v[118:119], v131 offset:22528\n\t"
  "s_waitcnt lgkmcnt(12)\n\t"
  "v_mfma_f32_16x16x32_bf16 v[0:3], v[96:99], v[64:67], v[0:3]\n\t"
  "ds_read_b64_tr_b16 v[120:121], v132 offset:16384\n\t"
  "s_waitcnt lgkmcnt(11)\n\t"
  "v_mfma_f32_16x16x32_bf16 v[0:3], v[100:103], v[68:71], v[0:3]\n\t"
  "ds_read_b64_tr_b16 v[122:123], v132 offset:18432\n\t"
  "s_waitcnt lgkmcnt(10)\n\t"
  "v_mfma_f32_16x16x32_bf16 v[16:19], v[100:103], v[76:79], v[16:19]\n\t"
  "ds_read_b64_tr_b16 v[124:125], v132 offset:20480\n\t"
  "v_mfma_f32_16x16x32_bf16 v[16:19], v[96:99], v[72:75], v[16:19]\n\t"
  "ds_read_b64_tr_b16 v[126:127], v132 offset:22528\n\t"
  "s_waitcnt lgkmcnt(10)\n\t"
  "v_mfma_f32_16x16x32_bf16 v[20:23], v[104:107], v[72:75], v[20:23]\n\t"
  "ds_read_b128 v[80:83], v128 offset:36864\n\t"
  "s_waitcnt lgkmcnt(9)\n\t"
  "v_mfma_f32_16x16x32_bf16 v[20:23], v[108:111], v[76:79], v[20:23]\n\t"
  "ds_read_b128 v[84:87], v128 offset:37888\n\t"
  "v_mfma_f32_16x16x32_bf16 v[4:7], v[108:111], v[68:71], v[4:7]\n\t"
  "ds_read_b128 v[88:91], v128 offset:38912\n\t"
  "v_mfma_f32_16x16x32_bf16 v[4:7], v[104:107], v[64:67], v[4:7]\n\t"
  "ds_read_b128 v[92:95], v128 offset:39936\n\t"
  "s_waitcnt lgkmcnt(8)\n\t"
  "v_mfma_f32_16x16x32_bf16 v[8:11], v[112:115], v[64:67], v[8:11]\n\t"
  "v_mfma_f32_16x16x32_bf16 v[8:11], v[116:119], v[68:71], v[8:11]\n\t"
  "v_mfma_f32_16x16x32_bf16 v[24:27], v[116:119], v[76:79], v[24:27]\n\t"
  "v_mfma_f32_16x16x32_bf16 v[24:27], v[112:115], v[72:75], v[24:27]\n\t"
  "s_waitcnt lgkmcnt(4)\n\t"
  "v_mfma_f32_16x16x32_bf16 v[28:31], v[120:123], v[72:75], v[28:31]\n\t"
  "v_mfma_f32_16x16x32_bf16 v[28:31], v[124:127], v[76:79], v[28:31]\n\t"
  "v_mfma_f32_16x16x32_bf16 v[12:15], v[124:127], v[68:71], v[12:15]\n\t"
  "v_mfma_f32_16x16x32_bf16 v[12:15], v[120:123], v[64:67], v[12:15]\n\t"
  "s_waitcnt lgkmcnt(0)\n\t"
  "s_mov_b32 s40, %[ntr]\n\t"
  ".p2align 6\n\t"
  "LPT%=:\n\t"
  "s_barrier\n\t"
  "v_mfma_f32_16x16x32_bf16 v[32:35], v[96:99], v[80:83], v[32:35]\n\t"
  "ds_read_b128 v[64:67], v128 offset:0\n\t"
  "v_mfma_f32_16x16x32_bf16 v[32:35], v[100:103], v[84:87], v[32:35]\n\t"
  "ds_read_b128 v[68:71], v128 offset:1024\n\t"
  "v_mfma_f32_16x16x32_bf16 v[48:51], v[100:103], v[92:95], v[48:51]\n\t"
  "ds_read_b128 v[72:75], v128 offset:2048\n\t"
  "v_mfma_f32_16x16x32_bf16 v[48:51], v[96:99], v[88:91], v[48:51]\n\t"
  "ds_read_b128 v[76:79], v128 offset:3072\n\t"
  "v_mfma_f32_16x16x32_bf16 v[52:55], v[104:107], v[88:91], v[52:55]\n\t"
  "ds_read_b64_tr_b16 v[96:97], v129 offset:49152\n\t"
  "v_mfma_f32_16x16x32_bf16 v[52:55], v[108:111], v[92:95], v[52:55]\n\t"
  "ds_read_b64_tr_b16 v[98:99], v129 offset:51200\n\t"
  "v_mfma_f32_16x16x32_bf16 v[36:39], v[108:111], v[84:87], v[36:39]\n\t"
  "ds_read_b64_tr_b16 v[100:101], v129 offset:53248\n\t"
  "v_mfma_f32_16x16x32_bf16 v[36:39], v[104:107], v[80:83], v[36:39]\n\t"
  "ds_read_b64_tr_b16 v[102:103], v129 offset:55296\n\t"
  "v_mfma_f32_16x16x32_bf16 v[40:43], v[112:115], v[80:83], v[40:43]\n\t"
  "ds_read_b64_tr_b16 v[104:105], v130 offset:49152\n\t"
  "v_mfma_f32_16x16x32_bf16 v[40:43], v[116:119], v[84:87], v[40:43]\n\t"
  "ds_read_b64_tr_b16 v[106:107], v130 offset:51200\n\t"
  "v_mfma_f32_16x16x32_bf16 v[56:59], v[116:119], v[92:95], v[56:59]\n\t"
  "ds_read_b64_tr_b16 v[108:109], v130 offset:53248\n\t"
  "v_mfma_f32_16x16x32_bf16 v[56:59], v[112:115], v[88:91], v[56:59]\n\t"
  "ds_read_b64_tr_b16 v[110:111], v130 offset:55296\n\t"
  "v_mfma_f32_16x16x32_bf16 v[60:63], v[120:123], v[88:91], v[60:63]\n\t"
  "ds_read_b64_tr_b16 v[112:113], v131 offset:49152\n\t"
  "v_mfma_f32_16x16x32_bf16 v[60:63], v[124:127], v[92:95], v[60:63]\n\t"
  "ds_read_b64_tr_b16 v[114:115], v131 offset:51200\n\t"
  "v_mfma_f32_16x16x32_bf16 v[44:47], v[124:127], v[84:87], v[44:47]\n\t"
  "ds_read_b64_tr_b16 v[116:117], v131 offset:53248\n\t"
  "v_mfma_f32_16x16x32_bf16 v[44:47], v[120:123], v[80:83], v[44:47]\n\t"
  "ds_read_b64_tr_b16 v[118:119], v131 offset:55296\n\t"
  "s_waitcnt lgkmcnt(10)\n\t"
  "v_mfma_f32_16x16x32_bf16 v[0:3], v[96:99], v[64:67], v[0:3]\n\t"
  "ds_read_b64_tr_b16 v[120:121], v132 offset:49152\n\t"
  "s_waitcnt lgkmcnt(9)\n\t"
  "v_mfma_f32_16x16x32_bf16 v[0:3], v[100:103], v[68:71], v[0:3]\n\t"
  "ds_read_b64_tr_b16 v[122:123], v132 offset:51200\n\t"
  "v_mfma_f32_16x16x32_bf16 v[16:19], v[100:103], v[76:79], v[16:19]\n\t"
  "ds_read_b64_tr_b16 v[124:125], v132 offset:53248\n\t"
  "v_mfma_f32_16x16x32_bf16 v[16:19], v[96:99], v[72:75], v[16:19]\n\t"
  "ds_read_b64_tr_b16 v[126:127], v132 offset:55296\n\t"
  "s_waitcnt lgkmcnt(10)\n\t"
  "v_mfma_f32_16x16x32_bf16 v[20:23], v[104:107], v[72:75], v[20:23]\n\t"
  "ds_read_b128 v[80:83], v128 offset:4096\n\t"
  "s_waitcnt lgkmcnt(9)\n\t"
  "v_mfma_f32_16x16x32_bf16 v[20:23], v[108:111], v[76:79], v[20:23]\n\t"
  "ds_read_b128 v[84:87], v128 offset:5120\n\t"
  "v_mfma_f32_16x16x32_bf16 v[4:7], v[108:111], v[68:71], v[4:7]\n\t"
  "ds_read_b128 v[88:91], v128 offset:6144\n\t"
  "v_mfma_f32_16x16x32_bf16 v[4:7], v[104:107], v[64:67], v[4:7]\n\t"
  "ds_read_b128 v[92:95], v128 offset:7168\n\t"
  "s_waitcnt lgkmcnt(8)\n\t"
  "v_mfma_f32_16x16x32_bf16 v[8:11], v[112:115], v[64:67], v[8:11]\n\t"
  "v_mfma_f32_16x16x32_bf16 v[8:11], v[116:119], v[68:71], v[8:11]\n\t"
  "v_mfma_f32_16x16x32_bf16 v[24:27], v[116:119], v[76:79], v[24:27]\n\t"
  "v_mfma_f32_16x16x32_bf16 v[24:27], v[112:115], v[72:75], v[24:27]\n\t"
  "s_waitcnt lgkmcnt(4)\n\t"
  "v_mfma_f32_16x16x32_bf16 v[28:31], v[120:123], v[72:75], v[28:31]\n\t"
  "v_mfma_f32_16x16x32_bf16 v[28:31], v[124:127], v[76:79], v[28:31]\n\t"
  "v_mfma_f32_16x16x32_bf16 v[12:15], v[124:127], v[68:71], v[12:15]\n\t"
  "v_mfma_f32_16x16x32_bf16 v[12:15], v[120:123], v[64:67], v[12:15]\n\t"
  "s_waitcnt lgkmcnt(0)\n\t"
  "s_barrier\n\t"
  "v_mfma_f32_16x16x32_bf16 v[32:35], v[96:99], v[80:83], v[32:35]\n\t"
  "ds_read_b128 v[64:67], v128 offset:49152\n\t"
  "v_mfma_f32_16x16x32_bf16 v[32:35], v[100:103], v[84:87], v[32:35]\n\t"
  "ds_read_b128 v[68:71], v128 offset:50176\n\t"
  "v_mfma_f32_16x16x32_bf16 v[48:51], v[100:103], v[92:95], v[48:51]\n\t"
  "ds_read_b128 v[72:75], v128 offset:51200\n\t"
  "v_mfma_f32_16x16x32_bf16 v[48:51], v[96:99], v[88:91], v[48:51]\n\t"
  "ds_read_b128 v[76:79], v128 offset:52224\n\t"
  "v_mfma_f32_16x16x32_bf16 v[52:55], v[104:107], v[88:91], v[52:55]\n\t"
  "ds_read_b64_tr_b16 v[96:97], v129 offset:0\n\t"
  "v_mfma_f32_16x16x32_bf16 v[52:55], v[108:111], v[92:95], v[52:55]\n\t"
  "ds_read_b64_tr_b16 v[98:99], v129 offset:2048\n\t"
  "v_mfma_f32_16x16x32_bf16 v[36:39], v[108:111], v[84:87], v[36:39]\n\t"
  "ds_read_b64_tr_b16 v[100:101], v129 offset:4096\n\t"
  "v_mfma_f32_16x16x32_bf16 v[36:39], v[104:107], v[80:83], v[36:39]\n\t"
  "ds_read_b64_tr_b16 v[102:103], v129 offset:6144\n\t"
  "v_mfma_f32_16x16x32_bf16 v[40:43], v[112:115], v[80:83], v[40:43]\n\t"
  "ds_read_b64_tr_b16 v[104:105], v130 offset:0\n\t"
  "v_mfma_f32_16x16x32_bf16 v[40:43], v[116:119], v[84:87], v[40:43]\n\t"
  "ds_read_b64_tr_b16 v[106:107], v130 offset:2048\n\t"
  "v_mfma_f32_16x16x32_bf16 v[56:59], v[116:119], v[92:95], v[56:59]\n\t"
  "ds_read_b64_tr_b16 v[108:109], v130 offset:4096\n\t"
  "v_mfma_f32_16x16x32_bf16 v[56:59], v[112:115], v[88:91], v[56:59]\n\t"
  "ds_read_b64_tr_b16 v[110:111], v130 offset:6144\n\t"
  "v_mfma_f32_16x16x32_bf16 v[60:63], v[120:123], v[88:91], v[60:63]\n\t"
  "ds_read_b64_tr_b16 v[112:113], v131 offset:0\n\t"
  "v_mfma_f32_16x16x32_bf16 v[60:63], v[124:127], v[92:95], v[60:63]\n\t"
  "ds_read_b64_tr_b16 v[114:115], v131 offset:2048\n\t"
  "v_mfma_f32_16x16x32_bf16 v[44:47], v[124:127], v[84:87], v[44:47]\n\t"
  "ds_read_b64_tr_b16 v[116:117], v131 offset:4096\n\t"
  "v_mfma_f32_16x16x32_bf16 v[44:47], v[120:123], v[80:83], v[44:47]\n\t"
  "ds_read_b64_tr_b16 v[118:119], v131 offset:6144\n\t"
  "s_waitcnt lgkmcnt(10)\n\t"
  "v_mfma_f32_16x16x32_bf16 v[0:3], v[96:99], v[64:67], v[0:3]\n\t"
  "ds_read_b64_tr_b16 v[120:121], v132 offset:0\n\t"
  "s_waitcnt lgkmcnt(9)\n\t"
  "v_mfma_f32_16x16x32_bf16 v[0:3], v[100:103], v[68:71], v[0:3]\n\t"
  "ds_read_b64_tr_b16 v[122:123], v132 offset:2048\n\t"
  "v_mfma_f32_16x16x32_bf16 v[16:19], v[100:103], v[76:79], v[16:19]\n\t"
  "ds_read_b64_tr_b16 v[124:125], v132 offset:4096\n\t"
  "v_mfma_f32_16x16x32_bf16 v[16:19], v[96:99], v[72:75], v[16:19]\n\t"
  "ds_read_b64_tr_b16 v[126:127], v132 offset:6144\n\t"
  "s_waitcnt lgkmcnt(10)\n\t"
  "v_mfma_f32_16x16x32_bf16 v[20:23], v[104:107], v[72:75], v[20:23]\n\t"
  "ds_read_b128 v[80:83], v128 offset:53248\n\t"
  "s_waitcnt lgkmcnt(9)\n\t"
  "v_mfma_f32_16x16x32_bf16 v[20:23], v[108:111], v[76:79], v[20:23]\n\t"
  "ds_read_b128 v[84:87], v128 offset:54272\n\t"
  "v_mfma_f32_16x16x32_bf16 v[4:7], v[108:111], v[68:71], v[4:7]\n\t"
  "ds_read_b128 v[88:91], v128 offset:55296\n\t"
  "v_mfma_f32_16x16x32_bf16 v[4:7], v[104:107], v[64:67], v[4:7]\n\t"
  "ds_read_b128 v[92:95], v128 offset:56320\n\t"
  "s_waitcnt lgkmcnt(8)\n\t"
  "v_mfma_f32_16x16x32_bf16 v[8:11], v[112:115], v[64:67], v[8:11]\n\t"
  "v_mfma_f32_16x16x32_bf16 v[8:11], v[116:119], v[68:71], v[8:11]\n\t"
  "v_mfma_f32_16x16x32_bf16 v[24:27], v[116:119], v[76:79], v[24:27]\n\t"
  "v_mfma_f32_16x16x32_bf16 v[24:27], v[112:115], v[72:75], v[24:27]\n\t"
  "s_waitcnt lgkmcnt(4)\n\t"
  "v_mfma_f32_16x16x32_bf16 v[28:31], v[120:123], v[72:75], v[28:31]\n\t"
  "v_mfma_f32_16x16x32_bf16 v[28:31], v[124:127], v[76:79], v[28:31]\n\t"
  "v_mfma_f32_16x16x32_bf16 v[12:15], v[124:127], v[68:71], v[12:15]\n\t"
  "v_mfma_f32_16x16x32_bf16 v[12:15], v[120:123], v[64:67], v[12:15]\n\t"
  "s_waitcnt lgkmcnt(0)\n\t"
  "s_barrier\n\t"
  "v_mfma_f32_16x16x32_bf16 v[32:35], v[96:99], v[80:83], v[32:35]\n\t"
  "ds_read_b128 v[64:67], v128 offset:16384\n\t"
  "v_mfma_f32_16x16x32_bf16 v[32:35], v[100:103], v[84:87], v[32:35]\n\t"
  "ds_read_b128 v[68:71], v128 offset:17408\n\t"
  "v_mfma_f32_16x16x32_bf16 v[48:51], v[100:103], v[92:95], v[48:51]\n\t"
  "ds_read_b128 v[72:75], v128 offset:18432\n\t"
  "v_mfma_f32_16x16x32_bf16 v[48:51], v[96:99], v[88:91], v[48:51]\n\t"
  "ds_read_b128 v[76:79], v128 offset:19456\n\t"
  "v_mfma_f32_16x16x32_bf16 v[52:55], v[104:107], v[88:91], v[52:55]\n\t"
  "ds_read_b64_tr_b16 v[96:97], v129 offset:32768\n\t"
  "v_mfma_f32_16x16x32_bf16 v[52:55], v[108:111], v[92:95], v[52:55]\n\t"
  "ds_read_b64_tr_b16 v[98:99], v129 offset:34816\n\t"
  "v_mfma_f32_16x16x32_bf16 v[36:39], v[108:111], v[84:87], v[36:39]\n\t"
  "ds_read_b64_tr_b16 v[100:101], v129 offset:36864\n\t"
  "v_mfma_f32_16x16x32_bf16 v[36:39], v[104:107], v[80:83], v[36:39]\n\t"
  "ds_read_b64_tr_b16 v[102:103], v129 offset:38912\n\t"
  "v_mfma_f32_16x16x32_bf16 v[40:43], v[112:115], v[80:83], v[40:43]\n\t"
  "ds_read_b64_tr_b16 v[104:105], v130 offset:32768\n\t"
  "v_mfma_f32_16x16x32_bf16 v[40:43], v[116:119], v[84:87], v[40:43]\n\t"
  "ds_read_b64_tr_b16 v[106:107], v130 offset:34816\n\t"
  "v_mfma_f32_16x16x32_bf16 v[56:59], v[116:119], v[92:95], v[56:59]\n\t"
  "ds_read_b64_tr_b16 v[108:109], v130 offset:36864\n\t"
  "v_mfma_f32_16x16x32_bf16 v[56:59], v[112:115], v[88:91], v[56:59]\n\t"
  "ds_read_b64_tr_b16 v[110:111], v130 offset:38912\n\t"
  "v_mfma_f32_16x16x32_bf16 v[60:63], v[120:123], v[88:91], v[60:63]\n\t"
  "ds_read_b64_tr_b16 v[112:113], v131 offset:32768\n\t"
  "v_mfma_f32_16x16x32_bf16 v[60:63], v[124:127], v[92:95], v[60:63]\n\t"
  "ds_read_b64_tr_b16 v[114:115], v131 offset:34816\n\t"
  "v_mfma_f32_16x16x32_bf16 v[44:47], v[124:127], v[84:87], v[44:47]\n\t"
  "ds_read_b64_tr_b16 v[116:117], v131 offset:36864\n\t"
  "v_mfma_f32_16x16x32_bf16 v[44:47], v[120:123], v[80:83], v[44:47]\n\t"
  "ds_read_b64_tr_b16 v[118:119], v131 offset:38912\n\t"
  "s_waitcnt lgkmcnt(10)\n\t"
  "v_mfma_f32_16x16x32_bf16 v[0:3], v[96:99], v[64:67], v[0:3]\n\t"
  "ds_read_b64_tr_b16 v[120:121], v132 offset:32768\n\t"
  "s_waitcnt lgkmcnt(9)\n\t"
  "v_mfma_f32_16x16x32_bf16 v[0:3], v[100:103], v[68:71], v[0:3]\n\t"
  "ds_read_b64_tr_b16 v[122:123], v132 offset:34816\n\t"
  "v_mfma_f32_16x16x32_bf16 v[16:19], v[100:103], v[76:79], v[16:19]\n\t"
  "ds_read_b64_tr_b16 v[124:125], v132 offset:36864\n\t"
  "v_mfma_f32_16x16x32_bf16 v[16:19], v[96:99], v[72:75], v[16:19]\n\t"
  "ds_read_b64_tr_b16 v[126:127], v132 offset:38912\n\t"
  "s_waitcnt lgkmcnt(10)\n\t"
  "v_mfma_f32_16x16x32_bf16 v[20:23], v[104:107], v[72:75], v[20:23]\n\t"
  "ds_read_b128 v[80:83], v128 offset:20480\n\t"
  "s_waitcnt lgkmcnt(9)\n\t"
  "v_mfma_f32_16x16x32_bf16 v[20:23], v[108:111], v[76:79], v[20:23]\n\t"
  "ds_read_b128 v[84:87], v128 offset:21504\n\t"
  "v_mfma_f32_16x16x32_bf16 v[4:7], v[108:111], v[68:71], v[4:7]\n\t"
  "ds_read_b128 v[88:91], v128 offset:22528\n\t"
  "v_mfma_f32_16x16x32_bf16 v[4:7], v[104:107], v[64:67], v[4:7]\n\t"
  "ds_read_b128 v[92:95], v128 offset:23552\n\t"
  "s_waitcnt lgkmcnt(8)\n\t"
  "v_mfma_f32_16x16x32_bf16 v[8:11], v[112:115], v[64:67], v[8:11]\n\t"
  "v_mfma_f32_16x16x32_bf16 v[8:11], v[116:119], v[68:71], v[8:11]\n\t"
  "v_mfma_f32_16x16x32_bf16 v[24:27], v[116:119], v[76:79], v[24:27]\n\t"
  "v_mfma_f32_16x16x32_bf16 v[24:27], v[112:115], v[72:75], v[24:27]\n\t"
  "s_waitcnt lgkmcnt(6)\n\t"
  "v_mfma_f32_16x16x32_bf16 v[28:31], v[120:123], v[72:75], v[28:31]\n\t"
  "s_sub_u32 s40, s40, 1\n\t"
  "s_waitcnt lgkmcnt(4)\n\t"
  "v_mfma_f32_16x16x32_bf16 v[28:31], v[124:127], v[76:79], v[28:31]\n\t"
  "s_cmp_eq_u32 s40, 0\n\t"
  "v_mfma_f32_16x16x32_bf16 v[12:15], v[124:127], v[68:71], v[12:15]\n\t"
  "v_mfma_f32_16x16x32_bf16 v[12:15], v[120:123], v[64:67], v[12:15]\n\t"
  "s_waitcnt lgkmcnt(0)\n\t"
  "s_cbranch_scc1 LET%=\n\t"
  "s_barrier\n\t"
  "v_mfma_f32_16x16x32_bf16 v[32:35], v[96:99], v[80:83], v[32:35]\n\t"
  "ds_read_b128 v[64:67], v128 offset:32768\n\t"
  "v_mfma_f32_16x16x32_bf16 v[32:35], v[100:103], v[84:87], v[32:35]\n\t"
  "ds_read_b128 v[68:71], v128 offset:33792\n\t"
  "v_mfma_f32_16x16x32_bf16 v[48:51], v[100:103], v[92:95], v[48:51]\n\t"
  "ds_read_b128 v[72:75], v128 offset:34816\n\t"
  "v_mfma_f32_16x16x32_bf16 v[48:51], v[96:99], v[88:91], v[48:51]\n\t"
  "ds_read_b128 v[76:79], v128 offset:35840\n\t"
  "v_mfma_f32_16x16x32_bf16 v[52:55], v[104:107], v[88:91], v[52:55]\n\t"
  "ds_read_b64_tr_b16 v[96:97], v129 offset:16384\n\t"
  "v_mfma_f32_16x16x32_bf16 v[52:55], v[108:111], v[92:95], v[52:55]\n\t"
  "ds_read_b64_tr_b16 v[98:99], v129 offset:18432\n\t"
  "v_mfma_f32_16x16x32_bf16 v[36:39], v[108:111], v[84:87], v[36:39]\n\t"
  "ds_read_b64_tr_b16 v[100:101], v129 offset:20480\n\t"
  "v_mfma_f32_16x16x32_bf16 v[36:39], v[104:107], v[80:83], v[36:39]\n\t"
  "ds_read_b64_tr_b16 v[102:103], v129 offset:22528\n\t"
  "v_mfma_f32_16x16x32_bf16 v[40:43], v[112:115], v[80:83], v[40:43]\n\t"
  "ds_read_b64_tr_b16 v[104:105], v130 offset:16384\n\t"
  "v_mfma_f32_16x16x32_bf16 v[40:43], v[116:119], v[84:87], v[40:43]\n\t"
  "ds_read_b64_tr_b16 v[106:107], v130 offset:18432\n\t"
  "v_mfma_f32_16x16x32_bf16 v[56:59], v[116:119], v[92:95], v[56:59]\n\t"
  "ds_read_b64_tr_b16 v[108:109], v130 offset:20480\n\t"
  "v_mfma_f32_16x16x32_bf16 v[56:59], v[112:115], v[88:91], v[56:59]\n\t"
  "ds_read_b64_tr_b16 v[110:111], v130 offset:22528\n\t"
  "v_mfma_f32_16x16x32_bf16 v[60:63], v[120:123], v[88:91], v[60:63]\n\t"
  "ds_read_b64_tr_b16 v[112:113], v131 offset:16384\n\t"
  "v_mfma_f32_16x16x32_bf16 v[60:63], v[124:127], v[92:95], v[60:63]\n\t"
  "ds_read_b64_tr_b16 v[114:115], v131 offset:18432\n\t"
  "v_mfma_f32_16x16x32_bf16 v[44:47], v[124:127], v[84:87], v[44:47]\n\t"
  "ds_read_b64_tr_b16 v[116:117], v131 offset:20480\n\t"
  "v_mfma_f32_16x16x32_bf16 v[44:47], v[120:123], v[80:83], v[44:47]\n\t"
  "ds_read_b64_tr_b16 v[118:119], v131 offset:22528\n\t"
  "s_waitcnt lgkmcnt(10)\n\t"
  "v_mfma_f32_16x16x32_bf16 v[0:3], v[96:99], v[64:67], v[0:3]\n\t"
  "ds_read_b64_tr_b16 v[120:121], v132 offset:16384\n\t"
  "s_waitcnt lgkmcnt(9)\n\t"
  "v_mfma_f32_16x16x32_bf16 v[0:3], v[100:103], v[68:71], v[0:3]\n\t"
  "ds_read_b64_tr_b16 v[122:123], v132 offset:18432\n\t"
  "v_mfma_f32_16x16x32_bf16 v[16:19], v[100:103], v[76:79], v[16:19]\n\t"
  "ds_read_b64_tr_b16 v[124:125], v132 offset:20480\n\t"
  "v_mfma_f32_16x16x32_bf16 v[16:19], v[96:99], v[72:75], v[16:19]\n\t"
  "ds_read_b64_tr_b16 v[126:127], v132 offset:22528\n\t"
  "s_waitcnt lgkmcnt(10)\n\t"
  "v_mfma_f32_16x16x32_bf16 v[20:23], v[104:107], v[72:75], v[20:23]\n\t"
  "ds_read_b128 v[80:83], v128 offset:36864\n\t"
  "s_waitcnt lgkmcnt(9)\n\t"
  "v_mfma_f32_16x16x32_bf16 v[20:23], v[108:111], v[76:79], v[20:23]\n\t"
  "ds_read_b128 v[84:87], v128 offset:37888\n\t"
  "v_mfma_f32_16x16x32_bf16 v[4:7], v[108:111], v[68:71], v[4:7]\n\t"
  "ds_read_b128 v[88:91], v128 offset:38912\n\t"
  "v_mfma_f32_16x16x32_bf16 v[4:7], v[104:107], v[64:67], v[4:7]\n\t"
  "ds_read_b128 v[92:95], v128 offset:39936\n\t"
  "s_waitcnt lgkmcnt(8)\n\t"
  "v_mfma_f32_16x16x32_bf16 v[8:11], v[112:115], v[64:67], v[8:11]\n\t"
  "v_mfma_f32_16x16x32_bf16 v[8:11], v[116:119], v[68:71], v[8:11]\n\t"
  "v_mfma_f32_16x16x32_bf16 v[24:27], v[116:119], v[76:79], v[24:27]\n\t"
  "v_mfma_f32_16x16x32_bf16 v[24:27], v[112:115], v[72:75], v[24:27]\n\t"
  "s_waitcnt lgkmcnt(4)\n\t"
  "v_mfma_f32_16x16x32_bf16 v[28:31], v[120:123], v[72:75], v[28:31]\n\t"
  "v_mfma_f32_16x16x32_bf16 v[28:31], v[124:127], v[76:79], v[28:31]\n\t"
  "v_mfma_f32_16x16x32_bf16 v[12:15], v[124:127], v[68:71], v[12:15]\n\t"
  "v_mfma_f32_16x16x32_bf16 v[12:15], v[120:123], v[64:67], v[12:15]\n\t"
  "s_waitcnt lgkmcnt(0)\n\t"
  "s_branch LPT%=\n\t"
  "LET%=:\n\t"
  "s_mov_b32 s40, %[nrow]\n\t"
  ".p2align 6\n\t"
  "LPR%=:\n\t"
  "s_barrier\n\t"
  "v_mfma_f32_16x16x32_bf16 v[32:35], v[96:99], v[80:83], v[32:35]\n\t"
  "ds_read_b128 v[64:67], v128 offset:32768\n\t"
  "v_mfma_f32_16x16x32_bf16 v[32:35], v[100:103], v[84:87], v[32:35]\n\t"
  "ds_read_b128 v[68:71], v128 offset:33792\n\t"
  "v_mfma_f32_16x16x32_bf16 v[48:51], v[100:103], v[92:95], v[48:51]\n\t"
  "ds_read_b128 v[72:75], v128 offset:34816\n\t"
  "v_mfma_f32_16x16x32_bf16 v[48:51], v[96:99], v[88:91], v[48:51]\n\t"
  "ds_read_b128 v[76:79], v128 offset:35840\n\t"
  "v_mfma_f32_16x16x32_bf16 v[52:55], v[104:107], v[88:91], v[52:55]\n\t"
  "ds_read_b128 v[96:99], v149 offset:16384\n\t"
  "v_mfma_f32_16x16x32_bf16 v[52:55], v[108:111], v[92:95], v[52:55]\n\t"
  "ds_read_b128 v[100:103], v150 offset:16384\n\t"
  "v_mfma_f32_16x16x32_bf16 v[36:39], v[108:111], v[84:87], v[36:39]\n\t"
  "v_mfma_f32_16x16x32_bf16 v[36:39], v[104:107], v[80:83], v[36:39]\n\t"
  "v_mfma_f32_16x16x32_bf16 v[40:43], v[112:115], v[80:83], v[40:43]\n\t"
  "ds_read_b128 v[104:107], v149 offset:18432\n\t"
  "v_mfma_f32_16x16x32_bf16 v[40:43], v[116:119], v[84:87], v[40:43]\n\t"
  "ds_read_b128 v[108:111], v150 offset:18432\n\t"
  "v_mfma_f32_16x16x32_bf16 v[56:59], v[116:119], v[92:95], v[56:59]\n\t"
  "v_mfma_f32_16x16x32_bf16 v[56:59], v[112:115], v[88:91], v[56:59]\n\t"
  "v_mfma_f32_16x16x32_bf16 v[60:63], v[120:123], v[88:91], v[60:63]\n\t"
  "ds_read_b128 v[112:115], v149 offset:20480\n\t"
  "v_mfma_f32_16x16x32_bf16 v[60:63], v[124:127], v[92:95], v[60:63]\n\t"
  "ds_read_b128 v[116:119], v150 offset:20480\n\t"
  "v_mfma_f32_16x16x32_bf16 v[44:47], v[124:127], v[84:87], v[44:47]\n\t"
  "v_mfma_f32_16x16x32_bf16 v[44:47], v[120:123], v[80:83], v[44:47]\n\t"
  "s_waitcnt lgkmcnt(5)\n\t"
  "v_mfma_f32_16x16x32_bf16 v[0:3], v[96:99], v[64:67], v[0:3]\n\t"
  "ds_read_b128 v[120:123], v149 offset:22528\n\t"
  "s_waitcnt lgkmcnt(5)\n\t"
  "v_mfma_f32_16x16x32_bf16 v[0:3], v[100:103], v[68:71], v[0:3]\n\t"
  "ds_read_b128 v[124:127], v150 offset:22528\n\t"
  "v_mfma_f32_16x16x32_bf16 v[16:19], v[100:103], v[76:79], v[16:19]\n\t"
  "ds_read_b128 v[80:83], v128 offset:36864\n\t"
  "v_mfma_f32_16x16x32_bf16 v[16:19], v[96:99], v[72:75], v[16:19]\n\t"
  "ds_read_b128 v[84:87], v128 offset:37888\n\t"
  "s_waitcnt lgkmcnt(7)\n\t"
  "v_mfma_f32_16x16x32_bf16 v[20:23], v[104:107], v[72:75], v[20:23]\n\t"
  "ds_read_b128 v[88:91], v128 offset:38912\n\t"
  "s_waitcnt lgkmcnt(7)\n\t"
  "v_mfma_f32_16x16x32_bf16 v[20:23], v[108:111], v[76:79], v[20:23]\n\t"
  "ds_read_b128 v[92:95], v128 offset:39936\n\t"
  "v_mfma_f32_16x16x32_bf16 v[4:7], v[108:111], v[68:71], v[4:7]\n\t"
  "v_mfma_f32_16x16x32_bf16 v[4:7], v[104:107], v[64:67], v[4:7]\n\t"
  "s_waitcnt lgkmcnt(6)\n\t"
  "v_mfma_f32_16x16x32_bf16 v[8:11], v[112:115], v[64:67], v[8:11]\n\t"
  "v_mfma_f32_16x16x32_bf16 v[8:11], v[116:119], v[68:71], v[8:11]\n\t"
  "v_mfma_f32_16x16x32_bf16 v[24:27], v[116:119], v[76:79], v[24:27]\n\t"
  "v_mfma_f32_16x16x32_bf16 v[24:27], v[112:115], v[72:75], v[24:27]\n\t"
  "s_waitcnt lgkmcnt(5)\n\t"
  "v_mfma_f32_16x16x32_bf16 v[28:31], v[120:123], v[72:75], v[28:31]\n\t"
  "s_sub_u32 s40, s40, 1\n\t"
  "s_waitcnt lgkmcnt(4)\n\t"
  "v_mfma_f32_16x16x32_bf16 v[28:31], v[124:127], v[76:79], v[28:31]\n\t"
  "s_cmp_eq_u32 s40, 0\n\t"
  "v_mfma_f32_16x16x32_bf16 v[12:15], v[124:127], v[68:71], v[12:15]\n\t"
  "v_mfma_f32_16x16x32_bf16 v[12:15], v[120:123], v[64:67], v[12:15]\n\t"
  "s_waitcnt lgkmcnt(0)\n\t"
  "s_cbranch_scc1 LER%=\n\t"
  "s_barrier\n\t"
  "v_mfma_f32_16x16x32_bf16 v[32:35], v[96:99], v[80:83], v[32:35]\n\t"
  "ds_read_b128 v[64:67], v128 offset:0\n\t"
  "v_mfma_f32_16x16x32_bf16 v[32:35], v[100:103], v[84:87], v[32:35]\n\t"
  "ds_read_b128 v[68:71], v128 offset:1024\n\t"
  "v_mfma_f32_16x16x32_bf16 v[48:51], v[100:103], v[92:95], v[48:51]\n\t"
  "ds_read_b128 v[72:75], v128 offset:2048\n\t"
  "v_mfma_f32_16x16x32_bf16 v[48:51], v[96:99], v[88:91], v[48:51]\n\t"
  "ds_read_b128 v[76:79], v128 offset:3072\n\t"
  "v_mfma_f32_16x16x32_bf16 v[52:55], v[104:107], v[88:91], v[52:55]\n\t"
  "ds_read_b128 v[96:99], v149 offset:49152\n\t"
  "v_mfma_f32_16x16x32_bf16 v[52:55], v[108:111], v[92:95], v[52:55]\n\t"
  "ds_read_b128 v[100:103], v150 offset:49152\n\t"
  "v_mfma_f32_16x16x32_bf16 v[36:39], v[108:111], v[84:87], v[36:39]\n\t"
  "v_mfma_f32_16x16x32_bf16 v[36:39], v[104:107], v[80:83], v[36:39]\n\t"
  "v_mfma_f32_16x16x32_bf16 v[40:43], v[112:115], v[80:83], v[40:43]\n\t"
  "ds_read_b128 v[104:107], v149 offset:51200\n\t"
  "v_mfma_f32_16x16x32_bf16 v[40:43], v[116:119], v[84:87], v[40:43]\n\t"
  "ds_read_b128 v[108:111], v150 offset:51200\n\t"
  "v_mfma_f32_16x16x32_bf16 v[56:59], v[116:119], v[92:95], v[56:59]\n\t"
  "v_mfma_f32_16x16x32_bf16 v[56:59], v[112:115], v[88:91], v[56:59]\n\t"
  "v_mfma_f32_16x16x32_bf16 v[60:63], v[120:123], v[88:91], v[60:63]\n\t"
  "ds_read_b128 v[112:115], v149 offset:53248\n\t"
  "v_mfma_f32_16x16x32_bf16 v[60:63], v[124:127], v[92:95], v[60:63]\n\t"
  "ds_read_b128 v[116:119], v150 offset:53248\n\t"
  "v_mfma_f32_16x16x32_bf16 v[44:47], v[124:127], v[84:87], v[44:47]\n\t"
  "v_mfma_f32_16x16x32_bf16 v[44:47], v[120:123], v[80:83], v[44:47]\n\t"
  "s_waitcnt lgkmcnt(5)\n\t"
  "v_mfma_f32_16x16x32_bf16 v[0:3], v[96:99], v[64:67], v[0:3]\n\t"
  "ds_read_b128 v[120:123], v149 offset:55296\n\t"
  "s_waitcnt lgkmcnt(5)\n\t"
  "v_mfma_f32_16x16x32_bf16 v[0:3], v[100:103], v[68:71], v[0:3]\n\t"
  "ds_read_b128 v[124:127], v150 offset:55296\n\t"
  "v_mfma_f32_16x16x32_bf16 v[16:19], v[100:103], v[76:79], v[16:19]\n\t"
  "ds_read_b128 v[80:83], v128 offset:4096\n\t"
  "v_mfma_f32_16x16x32_bf16 v[16:19], v[96:99], v[72:75], v[16:19]\n\t"
  "ds_read_b128 v[84:87], v128 offset:5120\n\t"
  "s_waitcnt lgkmcnt(7)\n\t"
  "v_mfma_f32_16x16x32_bf16 v[20:23], v[104:107], v[72:75], v[20:23]\n\t"
  "ds_read_b128 v[88:91], v128 offset:6144\n\t"
  "s_waitcnt lgkmcnt(7)\n\t"
  "v_mfma_f32_16x16x32_bf16 v[20:23], v[108:111], v[76:79], v[20:23]\n\t"
  "ds_read_b128 v[92:95], v128 offset:7168\n\t"
  "v_mfma_f32_16x16x32_bf16 v[4:7], v[108:111], v[68:71], v[4:7]\n\t"
  "v_mfma_f32_16x16x32_bf16 v[4:7], v[104:107], v[64:67], v[4:7]\n\t"
  "s_waitcnt lgkmcnt(6)\n\t"
  "v_mfma_f32_16x16x32_bf16 v[8:11], v[112:115], v[64:67], v[8:11]\n\t"
  "v_mfma_f32_16x16x32_bf16 v[8:11], v[116:119], v[68:71], v[8:11]\n\t"
  "v_mfma_f32_16x16x32_bf16 v[24:27], v[116:119], v[76:79], v[24:27]\n\t"
  "v_mfma_f32_16x16x32_bf16 v[24:27], v[112:115], v[72:75], v[24:27]\n\t"
  "s_waitcnt lgkmcnt(4)\n\t"
  "v_mfma_f32_16x16x32_bf16 v[28:31], v[120:123], v[72:75], v[28:31]\n\t"
  "v_mfma_f32_16x16x32_bf16 v[28:31], v[124:127], v[76:79], v[28:31]\n\t"
  "v_mfma_f32_16x16x32_bf16 v[12:15], v[124:127], v[68:71], v[12:15]\n\t"
  "v_mfma_f32_16x16x32_bf16 v[12:15], v[120:123], v[64:67], v[12:15]\n\t"
  "s_waitcnt lgkmcnt(0)\n\t"
  "s_barrier\n\t"
  "v_mfma_f32_16x16x32_bf16 v[32:35], v[96:99], v[80:83], v[32:35]\n\t"
  "ds_read_b128 v[64:67], v128 offset:49152\n\t"
  "v_mfma_f32_16x16x32_bf16 v[32:35], v[100:103], v[84:87], v[32:35]\n\t"
  "ds_read_b128 v[68:71], v128 offset:50176\n\t"
  "v_mfma_f32_16x16x32_bf16 v[48:51], v[100:103], v[92:95], v[48:51]\n\t"
  "ds_read_b128 v[72:75], v128 offset:51200\n\t"
  "v_mfma_f32_16x16x32_bf16 v[48:51], v[96:99], v[88:91], v[48:51]\n\t"
  "ds_read_b128 v[76:79], v128 offset:52224\n\t"
  "v_mfma_f32_16x16x32_bf16 v[52:55], v[104:107], v[88:91], v[52:55]\n\t"
  "ds_read_b128 v[96:99], v149 offset:0\n\t"
  "v_mfma_f32_16x16x32_bf16 v[52:55], v[108:111], v[92:95], v[52:55]\n\t"
  "ds_read_b128 v[100:103], v150 offset:0\n\t"
  "v_mfma_f32_16x16x32_bf16 v[36:39], v[108:111], v[84:87], v[36:39]\n\t"
  "v_mfma_f32_16x16x32_bf16 v[36:39], v[104:107], v[80:83], v[36:39]\n\t"
  "v_mfma_f32_16x16x32_bf16 v[40:43], v[112:115], v[80:83], v[40:43]\n\t"
  "ds_read_b128 v[104:107], v149 offset:2048\n\t"
  "v_mfma_f32_16x16x32_bf16 v[40:43], v[116:119], v[84:87], v[40:43]\n\t"
  "ds_read_b128 v[108:111], v150 offset:2048\n\t"
  "v_mfma_f32_16x16x32_bf16 v[56:59], v[116:119], v[92:95], v[56:59]\n\t"
  "v_mfma_f32_16x16x32_bf16 v[56:59], v[112:115], v[88:91], v[56:59]\n\t"
  "v_mfma_f32_16x16x32_bf16 v[60:63], v[120:123], v[88:91], v[60:63]\n\t"
  "ds_read_b128 v[112:115], v149 offset:4096\n\t"
  "v_mfma_f32_16x16x32_bf16 v[60:63], v[124:127], v[92:95], v[60:63]\n\t"
  "ds_read_b128 v[116:119], v150 offset:4096\n\t"
  "v_mfma_f32_16x16x32_bf16 v[44:47], v[124:127], v[84:87], v[44:47]\n\t"
  "v_mfma_f32_16x16x32_bf16 v[44:47], v[120:123], v[80:83], v[44:47]\n\t"
  "s_waitcnt lgkmcnt(5)\n\t"
  "v_mfma_f32_16x16x32_bf16 v[0:3], v[96:99], v[64:67], v[0:3]\n\t"
  "ds_read_b128 v[120:123], v149 offset:6144\n\t"
  "s_waitcnt lgkmcnt(5)\n\t"
  "v_mfma_f32_16x16x32_bf16 v[0:3], v[100:103], v[68:71], v[0:3]\n\t"
  "ds_read_b128 v[124:127], v150 offset:6144\n\t"
  "v_mfma_f32_16x16x32_bf16 v[16:19], v[100:103], v[76:79], v[16:19]\n\t"
  "ds_read_b128 v[80:83], v128 offset:53248\n\t"
  "v_mfma_f32_16x16x32_bf16 v[16:19], v[96:99], v[72:75], v[16:19]\n\t"
  "ds_read_b128 v[84:87], v128 offset:54272\n\t"
  "s_waitcnt lgkmcnt(7)\n\t"
  "v_mfma_f32_16x16x32_bf16 v[20:23], v[104:107], v[72:75], v[20:23]\n\t"
  "ds_read_b128 v[88:91], v128 offset:55296\n\t"
  "s_waitcnt lgkmcnt(7)\n\t"
  "v_mfma_f32_16x16x32_bf16 v[20:23], v[108:111], v[76:79], v[20:23]\n\t"
  "ds_read_b128 v[92:95], v128 offset:56320\n\t"
  "v_mfma_f32_16x16x32_bf16 v[4:7], v[108:111], v[68:71], v[4:7]\n\t"
  "v_mfma_f32_16x16x32_bf16 v[4:7], v[104:107], v[64:67], v[4:7]\n\t"
  "s_waitcnt lgkmcnt(6)\n\t"
  "v_mfma_f32_16x16x32_bf16 v[8:11], v[112:115], v[64:67], v[8:11]\n\t"
  "v_mfma_f32_16x16x32_bf16 v[8:11], v[116:119], v[68:71], v[8:11]\n\t"
  "v_mfma_f32_16x16x32_bf16 v[24:27], v[116:119], v[76:79], v[24:27]\n\t"
  "v_mfma_f32_16x16x32_bf16 v[24:27], v[112:115], v[72:75], v[24:27]\n\t"
  "s_waitcnt lgkmcnt(5)\n\t"
  "v_mfma_f32_16x16x32_bf16 v[28:31], v[120:123], v[72:75], v[28:31]\n\t"
  "s_sub_u32 s40, s40, 1\n\t"
  "s_waitcnt lgkmcnt(4)\n\t"
  "v_mfma_f32_16x16x32_bf16 v[28:31], v[124:127], v[76:79], v[28:31]\n\t"
  "s_cmp_eq_u32 s40, 0\n\t"
  "v_mfma_f32_16x16x32_bf16 v[12:15], v[124:127], v[68:71], v[12:15]\n\t"
  "v_mfma_f32_16x16x32_bf16 v[12:15], v[120:123], v[64:67], v[12:15]\n\t"
  "s_waitcnt lgkmcnt(0)\n\t"
  "s_cbranch_scc1 LER%=\n\t"
  "s_barrier\n\t"
  "v_mfma_f32_16x16x32_bf16 v[32:35], v[96:99], v[80:83], v[32:35]\n\t"
  "ds_read_b128 v[64:67], v128 offset:16384\n\t"
  "v_mfma_f32_16x16x32_bf16 v[32:35], v[100:103], v[84:87], v[32:35]\n\t"
  "ds_read_b128 v[68:71], v128 offset:17408\n\t"
  "v_mfma_f32_16x16x32_bf16 v[48:51], v[100:103], v[92:95], v[48:51]\n\t"
  "ds_read_b128 v[72:75], v128 offset:18432\n\t"
  "v_mfma_f32_16x16x32_bf16 v[48:51], v[96:99], v[88:91], v[48:51]\n\t"
  "ds_read_b128 v[76:79], v128 offset:19456\n\t"
  "v_mfma_f32_16x16x32_bf16 v[52:55], v[104:107], v[88:91], v[52:55]\n\t"
  "ds_read_b128 v[96:99], v149 offset:32768\n\t"
  "v_mfma_f32_16x16x32_bf16 v[52:55], v[108:111], v[92:95], v[52:55]\n\t"
  "ds_read_b128 v[100:103], v150 offset:32768\n\t"
  "v_mfma_f32_16x16x32_bf16 v[36:39], v[108:111], v[84:87], v[36:39]\n\t"
  "v_mfma_f32_16x16x32_bf16 v[36:39], v[104:107], v[80:83], v[36:39]\n\t"
  "v_mfma_f32_16x16x32_bf16 v[40:43], v[112:115], v[80:83], v[40:43]\n\t"
  "ds_read_b128 v[104:107], v149 offset:34816\n\t"
  "v_mfma_f32_16x16x32_bf16 v[40:43], v[116:119], v[84:87], v[40:43]\n\t"
  "ds_read_b128 v[108:111], v150 offset:34816\n\t"
  "v_mfma_f32_16x16x32_bf16 v[56:59], v[116:119], v[92:95], v[56:59]\n\t"
  "v_mfma_f32_16x16x32_bf16 v[56:59], v[112:115], v[88:91], v[56:59]\n\t"
  "v_mfma_f32_16x16x32_bf16 v[60:63], v[120:123], v[88:91], v[60:63]\n\t"
  "ds_read_b128 v[112:115], v149 offset:36864\n\t"
  "v_mfma_f32_16x16x32_bf16 v[60:63], v[124:127], v[92:95], v[60:63]\n\t"
  "ds_read_b128 v[116:119], v150 offset:36864\n\t"
  "v_mfma_f32_16x16x32_bf16 v[44:47], v[124:127], v[84:87], v[44:47]\n\t"
  "v_mfma_f32_16x16x32_bf16 v[44:47], v[120:123], v[80:83], v[44:47]\n\t"
  "s_waitcnt lgkmcnt(5)\n\t"
  "v_mfma_f32_16x16x32_bf16 v[0:3], v[96:99], v[64:67], v[0:3]\n\t"
  "ds_read_b128 v[120:123], v149 offset:38912\n\t"
  "s_waitcnt lgkmcnt(5)\n\t"
  "v_mfma_f32_16x16x32_bf16 v[0:3], v[100:103], v[68:71], v[0:3]\n\t"
  "ds_read_b128 v[124:127], v150 offset:38912\n\t"
  "v_mfma_f32_16x16x32_bf16 v[16:19], v[100:103], v[76:79], v[16:19]\n\t"
  "ds_read_b128 v[80:83], v128 offset:20480\n\t"
  "v_mfma_f32_16x16x32_bf16 v[16:19], v[96:99], v[72:75], v[16:19]\n\t"
  "ds_read_b128 v[84:87], v128 offset:21504\n\t"
  "s_waitcnt lgkmcnt(7)\n\t"
  "v_mfma_f32_16x16x32_bf16 v[20:23], v[104:107], v[72:75], v[20:23]\n\t"
  "ds_read_b128 v[88:91], v128 offset:22528\n\t"
  "s_waitcnt lgkmcnt(7)\n\t"
  "v_mfma_f32_16x16x32_bf16 v[20:23], v[108:111], v[76:79], v[20:23]\n\t"
  "ds_read_b128 v[92:95], v128 offset:23552\n\t"
  "v_mfma_f32_16x16x32_bf16 v[4:7], v[108:111], v[68:71], v[4:7]\n\t"
  "v_mfma_f32_16x16x32_bf16 v[4:7], v[104:107], v[64:67], v[4:7]\n\t"
  "s_waitcnt lgkmcnt(6)\n\t"
  "v_mfma_f32_16x16x32_bf16 v[8:11], v[112:115], v[64:67], v[8:11]\n\t"
  "v_mfma_f32_16x16x32_bf16 v[8:11], v[116:119], v[68:71], v[8:11]\n\t"
  "v_mfma_f32_16x16x32_bf16 v[24:27], v[116:119], v[76:79], v[24:27]\n\t"
  "v_mfma_f32_16x16x32_bf16 v[24:27], v[112:115], v[72:75], v[24:27]\n\t"
  "s_waitcnt lgkmcnt(4)\n\t"
  "v_mfma_f32_16x16x32_bf16 v[28:31], v[120:123], v[72:75], v[28:31]\n\t"
  "v_mfma_f32_16x16x32_bf16 v[28:31], v[124:127], v[76:79], v[28:31]\n\t"
  "v_mfma_f32_16x16x32_bf16 v[12:15], v[124:127], v[68:71], v[12:15]\n\t"
  "v_mfma_f32_16x16x32_bf16 v[12:15], v[120:123], v[64:67], v[12:15]\n\t"
  "s_waitcnt lgkmcnt(0)\n\t"
  "s_branch LPR%=\n\t"
  "LER%=:\n\t"
  "s_cmp_lg_u32 %[isp], 0\n\t"
  "s_cbranch_scc1 FP%=\n\t"
  "s_barrier\n\t"
  "v_mfma_f32_16x16x32_bf16 v[32:35], v[96:99], v[80:83], v[32:35]\n\t"
  "ds_read_b128 v[160:163], v142 offset:0\n\t"
  "v_mfma_f32_16x16x32_bf16 v[32:35], v[100:103], v[84:87], v[32:35]\n\t"
  "ds_read_b128 v[164:167], v142 offset:1024\n\t"
  "v_mfma_f32_16x16x32_bf16 v[48:51], v[100:103], v[92:95], v[48:51]\n\t"
  "ds_read_b128 v[168:171], v142 offset:2048\n\t"
  "v_mfma_f32_16x16x32_bf16 v[48:51], v[96:99], v[88:91], v[48:51]\n\t"
  "ds_read_b128 v[172:175], v142 offset:3072\n\t"
  "v_mfma_f32_16x16x32_bf16 v[52:55], v[104:107], v[88:91], v[52:55]\n\t"
  "ds_read_b128 v[176:179], v142 offset:4096\n\t"
  "v_mfma_f32_16x16x32_bf16 v[52:55], v[108:111], v[92:95], v[52:55]\n\t"
  "ds_read_b128 v[180:183], v142 offset:5120\n\t"
  "v_mfma_f32_16x16x32_bf16 v[36:39], v[108:111], v[84:87], v[36:39]\n\t"
  "ds_read_b128 v[184:187], v142 offset:6144\n\t"
  "v_mfma_f32_16x16x32_bf16 v[36:39], v[104:107], v[80:83], v[36:39]\n\t"
  "ds_read_b128 v[188:191], v142 offset:7168\n\t"
  "s_waitcnt lgkmcnt(4)\n\t"
  "v_mfma_f32_16x16x32_bf16 v[0:3], v[134:137], v[160:163], v[0:3]\n\t"
  "v_mfma_f32_16x16x32_bf16 v[4:7], v[138:141], v[160:163], v[4:7]\n\t"
  "v_mfma_f32_16x16x32_bf16 v[8:11], v[134:137], v[164:167], v[8:11]\n\t"
  "v_mfma_f32_16x16x32_bf16 v[12:15], v[138:141], v[164:167], v[12:15]\n\t"
  "v_mfma_f32_16x16x32_bf16 v[16:19], v[134:137], v[168:171], v[16:19]\n\t"
  "v_mfma_f32_16x16x32_bf16 v[20:23], v[138:141], v[168:171], v[20:23]\n\t"
  "v_mfma_f32_16x16x32_bf16 v[24:27], v[134:137], v[172:175], v[24:27]\n\t"
  "v_mfma_f32_16x16x32_bf16 v[28:31], v[138:141], v[172:175], v[28:31]\n\t"
  "s_waitcnt lgkmcnt(0)\n\t"
  "v_mfma_f32_16x16x32_bf16 v[40:43], v[112:115], v[80:83], v[40:43]\n\t"
  "v_cvt_pk_bf16_f32 v192, v0, v1\n\t"
  "v_cvt_pk_bf16_f32 v193, v2, v3\n\t"
  "ds_write_b64 v143, v[192:193] offset:0\n\t"
  "v_mfma_f32_16x16x32_bf16 v[40:43], v[116:119], v[84:87], v[40:43]\n\t"
  "v_cvt_pk_bf16_f32 v194, v4, v5\n\t"
  "v_cvt_pk_bf16_f32 v195, v6, v7\n\t"
  "v_mfma_f32_16x16x32_bf16 v[56:59], v[116:119], v[92:95], v[56:59]\n\t"
  "ds_write_b64 v144, v[194:195] offset:0\n\t"
  "v_cvt_pk_bf16_f32 v196, v8, v9\n\t"
  "v_mfma_f32_16x16x32_bf16 v[56:59], v[112:115], v[88:91], v[56:59]\n\t"
  "v_cvt_pk_bf16_f32 v197, v10, v11\n\t"
  "ds_write_b64 v145, v[196:197] offset:0\n\t"
  "v_mfma_f32_16x16x32_bf16 v[60:63], v[120:123], v[88:91], v[60:63]\n\t"
  "v_cvt_pk_bf16_f32 v198, v12, v13\n\t"
  "v_cvt_pk_bf16_f32 v199, v14, v15\n\t"
  "v_mfma_f32_16x16x32_bf16 v[60:63], v[124:127], v[92:95], v[60:63]\n\t"
  "ds_write_b64 v146, v[198:199] offset:0\n\t"
  "ds_read_b128 v[224:227], v147 offset:0\n\t"
  "v_mfma_f32_16x16x32_bf16 v[44:47], v[124:127], v[84:87], v[44:47]\n\t"
  "ds_read_b128 v[228:231], v147 offset:1024\n\t"
  "v_cvt_pk_bf16_f32 v200, v16, v17\n\t"
  "v_mfma_f32_16x16x32_bf16 v[44:47], v[120:123], v[80:83], v[44:47]\n\t"
  "v_cvt_pk_bf16_f32 v201, v18, v19\n\t"
  "ds_write_b64 v143, v[200:201] offset:2048\n\t"
  "v_mfma_f32_16x16x32_bf16 v[32:35], v[134:137], v[176:179], v[32:35]\n\t"
  "v_cvt_pk_bf16_f32 v202, v20, v21\n\t"
  "v_cvt_pk_bf16_f32 v203, v22, v23\n\t"
  "ds_write_b64 v144, v[202:203] offset:2048\n\t"
  "v_mfma_f32_16x16x32_bf16 v[36:39], v[138:141], v[176:179], v[36:39]\n\t"
  "v_cvt_pk_bf16_f32 v204, v24, v25\n\t"
  "v_cvt_pk_bf16_f32 v205, v26, v27\n\t"
  "v_mfma_f32_16x16x32_bf16 v[48:51], v[134:137], v[184:187], v[48:51]\n\t"
  "ds_write_b64 v145, v[204:205] offset:2048\n\t"
  "v_cvt_pk_bf16_f32 v206, v28, v29\n\t"
  "v_mfma_f32_16x16x32_bf16 v[52:55], v[138:141], v[184:187], v[52:55]\n\t"
  "v_cvt_pk_bf16_f32 v207, v30, v31\n\t"
  "ds_write_b64 v146, v[206:207] offset:2048\n\t"
  "v_mfma_f32_16x16x32_bf16 v[40:43], v[134:137], v[180:183], v[40:43]\n\t"
  "ds_read_b128 v[232:235], v147 offset:2048\n\t"
  "ds_read_b128 v[236:239], v147 offset:3072\n\t"
  "v_mfma_f32_16x16x32_bf16 v[44:47], v[138:141], v[180:183], v[44:47]\n\t"
  "s_waitcnt lgkmcnt(7)\n\t"
  "s_mov_b32 s41, 0\n\t"
  "v_mfma_f32_16x16x32_bf16 v[56:59], v[134:137], v[188:191], v[56:59]\n\t"
  "buffer_store_dwordx4 v[224:227], v148, %[srd], s41 offen sc1\n\t"
  "s_waitcnt lgkmcnt(6)\n\t"
  "v_mfma_f32_16x16x32_bf16 v[60:63], v[138:141], v[188:191], v[60:63]\n\t"
  "s_mov_b32 s41, 16384\n\t"
  "buffer_store_dwordx4 v[228:231], v148, %[srd], s41 offen sc1\n\t"
  "v_cvt_pk_bf16_f32 v208, v32, v33\n\t"
  "v_cvt_pk_bf16_f32 v209, v34, v35\n\t"
  "ds_write_b64 v143, v[208:209] offset:4096\n\t"
  "v_cvt_pk_bf16_f32 v210, v36, v37\n\t"
  "v_cvt_pk_bf16_f32 v211, v38, v39\n\t"
  "ds_write_b64 v144, v[210:211] offset:4096\n\t"
  "v_cvt_pk_bf16_f32 v212, v40, v41\n\t"
  "v_cvt_pk_bf16_f32 v213, v42, v43\n\t"
  "ds_write_b64 v145, v[212:213] offset:4096\n\t"
  "v_cvt_pk_bf16_f32 v214, v44, v45\n\t"
  "v_cvt_pk_bf16_f32 v215, v46, v47\n\t"
  "ds_write_b64 v146, v[214:215] offset:4096\n\t"
  "ds_read_b128 v[240:243], v147 offset:4096\n\t"
  "ds_read_b128 v[244:247], v147 offset:5120\n\t"
  "s_waitcnt lgkmcnt(7)\n\t"
  "s_mov_b32 s41, 32768\n\t"
  "buffer_store_dwordx4 v[232:235], v148, %[srd], s41 offen sc1\n\t"
  "s_waitcnt lgkmcnt(6)\n\t"
  "s_mov_b32 s41, 49152\n\t"
  "buffer_store_dwordx4 v[236:239], v148, %[srd], s41 offen sc1\n\t"
  "v_cvt_pk_bf16_f32 v216, v48, v49\n\t"
  "v_cvt_pk_bf16_f32 v217, v50, v51\n\t"
  "ds_write_b64 v143, v[216:217] offset:6144\n\t"
  "v_cvt_pk_bf16_f32 v218, v52, v53\n\t"
  "v_cvt_pk_bf16_f32 v219, v54, v55\n\t"
  "ds_write_b64 v144, v[218:219] offset:6144\n\t"
  "v_cvt_pk_bf16_f32 v220, v56, v57\n\t"
  "v_cvt_pk_bf16_f32 v221, v58, v59\n\t"
  "ds_write_b64 v145, v[220:221] offset:6144\n\t"
  "v_cvt_pk_bf16_f32 v222, v60, v61\n\t"
  "v_cvt_pk_bf16_f32 v223, v62, v63\n\t"
  "ds_write_b64 v146, v[222:223] offset:6144\n\t"
  "ds_read_b128 v[248:251], v147 offset:6144\n\t"
  "ds_read_b128 v[252:255], v147 offset:7168\n\t"
  "s_waitcnt lgkmcnt(7)\n\t"
  "s_mov_b32 s41, 65536\n\t"
  "buffer_store_dwordx4 v[240:243], v148, %[srd], s41 offen sc1\n\t"
  "s_waitcnt lgkmcnt(6)\n\t"
  "s_mov_b32 s41, 81920\n\t"
  "buffer_store_dwordx4 v[244:247], v148, %[srd], s41 offen sc1\n\t"
  "s_waitcnt lgkmcnt(1)\n\t"
  "s_mov_b32 s41, 98304\n\t"
  "buffer_store_dwordx4 v[248:251], v148, %[srd], s41 offen sc1\n\t"
  "s_waitcnt lgkmcnt(0)\n\t"
  "s_mov_b32 s41, 114688\n\t"
  "buffer_store_dwordx4 v[252:255], v148, %[srd], s41 offen sc1\n\t"
  "s_branch FE%=\n\t"
  "FP%=:\n\t"
  "s_barrier\n\t"
  "v_mfma_f32_16x16x32_bf16 v[32:35], v[96:99], v[80:83], v[32:35]\n\t"
  "v_mfma_f32_16x16x32_bf16 v[32:35], v[100:103], v[84:87], v[32:35]\n\t"
  "v_mfma_f32_16x16x32_bf16 v[48:51], v[100:103], v[92:95], v[48:51]\n\t"
  "v_cvt_pk_bf16_f32 v72, v16, v17\n\t"
  "v_cvt_pk_bf16_f32 v73, v18, v19\n\t"
  "v_mfma_f32_16x16x32_bf16 v[48:51], v[96:99], v[88:91], v[48:51]\n\t"
  "v_cvt_pk_bf16_f32 v74, v20, v21\n\t"
  "v_mfma_f32_16x16x32_bf16 v[52:55], v[104:107], v[88:91], v[52:55]\n\t"
  "v_cvt_pk_bf16_f32 v75, v22, v23\n\t"
  "buffer_store_dwordx4 v[72:75], v151, %[spt], %[po0] offen offset:2048 sc1\n\t"
  "v_mfma_f32_16x16x32_bf16 v[52:55], v[108:111], v[92:95], v[52:55]\n\t"
  "v_cvt_pk_bf16_f32 v76, v24, v25\n\t"
  "v_mfma_f32_16x16x32_bf16 v[36:39], v[108:111], v[84:87], v[36:39]\n\t"
  "v_cvt_pk_bf16_f32 v77, v26, v27\n\t"
  "v_cvt_pk_bf16_f32 v78, v28, v29\n\t"
  "v_mfma_f32_16x16x32_bf16 v[36:39], v[104:107], v[80:83], v[36:39]\n\t"
  "v_cvt_pk_bf16_f32 v79, v30, v31\n\t"
  "v_mfma_f32_16x16x32_bf16 v[40:43], v[112:115], v[80:83], v[40:43]\n\t"
  "buffer_store_dwordx4 v[76:79], v151, %[spt], %[po0] offen offset:3072 sc1\n\t"
  "v_mfma_f32_16x16x32_bf16 v[40:43], v[116:119], v[84:87], v[40:43]\n\t"
  "v_cvt_pk_bf16_f32 v64, v0, v1\n\t"
  "v_cvt_pk_bf16_f32 v65, v2, v3\n\t"
  "v_mfma_f32_16x16x32_bf16 v[56:59], v[116:119], v[92:95], v[56:59]\n\t"
  "v_cvt_pk_bf16_f32 v66, v4, v5\n\t"
  "v_mfma_f32_16x16x32_bf16 v[56:59], v[112:115], v[88:91], v[56:59]\n\t"
  "v_cvt_pk_bf16_f32 v67, v6, v7\n\t"
  "buffer_store_dwordx4 v[64:67], v151, %[spt], %[po0] offen offset:0 sc1\n\t"
  "v_mfma_f32_16x16x32_bf16 v[60:63], v[120:123], v[88:91], v[60:63]\n\t"
  "v_cvt_pk_bf16_f32 v68, v8, v9\n\t"
  "v_mfma_f32_16x16x32_bf16 v[60:63], v[124:127], v[92:95], v[60:63]\n\t"
  "v_cvt_pk_bf16_f32 v69, v10, v11\n\t"
  "v_cvt_pk_bf16_f32 v70, v12, v13\n\t"
  "v_mfma_f32_16x16x32_bf16 v[44:47], v[124:127], v[84:87], v[44:47]\n\t"
  "v_cvt_pk_bf16_f32 v71, v14, v15\n\t"
  "v_mfma_f32_16x16x32_bf16 v[44:47], v[120:123], v[80:83], v[44:47]\n\t"
  "buffer_store_dwordx4 v[68:71], v151, %[spt], %[po0] offen offset:1024 sc1\n\t"
  "v_cvt_pk_bf16_f32 v8, v48, v49\n\t"
  "v_cvt_pk_bf16_f32 v9, v50, v51\n\t"
  "v_cvt_pk_bf16_f32 v10, v52, v53\n\t"
  "v_cvt_pk_bf16_f32 v11, v54, v55\n\t"
  "buffer_store_dwordx4 v[8:11], v151, %[spt], %[po1] offen offset:2048 sc1\n\t"
  "v_cvt_pk_bf16_f32 v12, v56, v57\n\t"
  "v_cvt_pk_bf16_f32 v13, v58, v59\n\t"
  "v_cvt_pk_bf16_f32 v14, v60, v61\n\t"
  "v_cvt_pk_bf16_f32 v15, v62, v63\n\t"
  "buffer_store_dwordx4 v[12:15], v151, %[spt], %[po1] offen offset:3072 sc1\n\t"
  "v_cvt_pk_bf16_f32 v0, v32, v33\n\t"
  "v_cvt_pk_bf16_f32 v1, v34, v35\n\t"
  "v_cvt_pk_bf16_f32 v2, v36, v37\n\t"
  "v_cvt_pk_bf16_f32 v3, v38, v39\n\t"
  "buffer_store_dwordx4 v[0:3], v151, %[spt], %[po1] offen offset:0 sc1\n\t"
  "v_cvt_pk_bf16_f32 v4, v40, v41\n\t"
  "v_cvt_pk_bf16_f32 v5, v42, v43\n\t"
  "v_cvt_pk_bf16_f32 v6, v44, v45\n\t"
  "v_cvt_pk_bf16_f32 v7, v46, v47\n\t"
  "buffer_store_dwordx4 v[4:7], v151, %[spt], %[po1] offen offset:1024 sc1\n\t"
  "s_waitcnt vmcnt(0)\n\t"
  "global_store_dwordx2 v[154:155], v[152:153], off sc1\n\t"
  "FE%=:\n\t"
  "s_nop 1\n\t"
  :: "{v128}"(sa), "{v129}"(sbt), "{v130}"(sbt ^ 32), "{v131}"(sbt ^ 64), "{v132}"(sbt ^ 96), "{v[134:137]}"(e0), "{v[138:141]}"(e1), "{v142}"(pvb), "{v143}"(tw0), "{v144}"(tw0 ^ 32), "{v145}"(tw0 ^ 64), "{v146}"(tw0 ^ 96), "{v147}"(trb), "{v148}"(vo), "{v149}"(sbr), "{v150}"(sbr ^ 64), "{v151}"(lane * 16), "{v[152:153]}"(tag), "{v[154:155]}"(pflag), [ntr]"s"(ntr), [nrow]"s"(nrow), [isp]"s"(isp), [srd]"s"(sOut), [spt]"s"(sPt), [po0]"s"(wid * 8192), [po1]"s"(wid * 8192 + 4096) : "memory","scc","s40","s41","v0","v1","v2","v3","v4","v5","v6","v7","v8","v9","v10","v11","v12","v13","v14","v15","v16","v17","v18","v19","v20","v21","v22","v23","v24","v25","v26","v27","v28","v29","v30","v31","v32","v33","v34","v35","v36","v37","v38","v39","v40","v41","v42","v43","v44","v45","v46","v47","v48","v49","v50","v51","v52","v53","v54","v55","v56","v57","v58","v59","v60","v61","v62","v63","v64","v65","v66","v67","v68","v69","v70","v71","v72","v73","v74","v75","v76","v77","v78","v79","v80","v81","v82","v83","v84","v85","v86","v87","v88","v89","v90","v91","v92","v93","v94","v95","v96","v97","v98","v99","v100","v101","v102","v103","v104","v105","v106","v107","v108","v109","v110","v111","v112","v113","v114","v115","v116","v117","v118","v119","v120","v121","v122","v123","v124","v125","v126","v127","v160","v161","v162","v163","v164","v165","v166","v167","v168","v169","v170","v171","v172","v173","v174","v175","v176","v177","v178","v179","v180","v181","v182","v183","v184","v185","v186","v187","v188","v189","v190","v191","v192","v193","v194","v195","v196","v197","v198","v199","v200","v201","v202","v203","v204","v205","v206","v207","v208","v209","v210","v211","v212","v213","v214","v215","v216","v217","v218","v219","v220","v221","v222","v223","v224","v225","v226","v227","v228","v229","v230","v231","v232","v233","v234","v235","v236","v237","v238","v239","v240","v241","v242","v243","v244","v245","v246","v247","v248","v249","v250","v251","v252","v253","v254","v255");
    }
  }
}

extern "C" void kernel_launch(void* const* d_in, const int* in_sizes, int n_in, void* d_out, int out_size, void* d_ws, size_t ws_size, hipStream_t stream) {
  (void)in_sizes; (void)n_in; (void)out_size; (void)ws_size;
  gmix<<<256, 512, 0, stream>>>((const u16*)d_in[0], (const u16*)d_in[1], (const u16*)d_in[2], (const u16*)d_in[4], (const u16*)d_in[5],
                                       (const u16*)d_in[6], (const u16*)d_in[7], (const u16*)d_in[8], (u16*)d_out, (char*)d_ws);
}
```
